```python
import torch

def _softmax(x, axis=-1):
    return torch.softmax(x, dim=axis)
def _gelu(x):
    return torch.nn.functional.gelu(x, approximate="tanh")
def _silu(x):
    return torch.nn.functional.silu(x)
def _sigmoid(x):
    return torch.sigmoid(x)
def _layernorm(x, axis=-1, eps=1e-5):
    m = x.mean(dim=axis, keepdim=True)
    v = x.var(dim=axis, keepdim=True, unbiased=False)
    return (x - m) / torch.sqrt(v + eps)
def _scatter_add(tgt, idx, src, axis=0):
    return tgt.clone().index_add_(axis, idx, src)
def _segment_sum(src, seg, n):
    out = torch.zeros((n,) + src.shape[1:], dtype=src.dtype)
    return out.index_add_(0, seg, src)
def _pad(x, pw):
    flat = tuple(v for pair in reversed(pw) for v in pair)
    return torch.nn.functional.pad(x, flat)

kernel_name = 'parallel_sum__ssm_conv_h3__dense_activation_silu__prompt'

def setup_inputs():

    _w = (lambda a: (lambda t: t.to(torch.bfloat16) if t.is_floating_point() else t)(torch.as_tensor(a)))
    def _A_setup():
        B, S, D = 1, 2048, 1024
        ks = 4
        return {
            "q": _w((lambda *s: torch.randn(*s, dtype=torch.bfloat16) * 0.05)(B, S, D)),
            "k": _w((lambda *s: torch.randn(*s, dtype=torch.bfloat16) * 0.2)(B, S, D)),
            "v": _w((lambda *s: torch.randn(*s, dtype=torch.bfloat16) * 0.2)(B, S, D)),
            "shift_k": _w((lambda *s: torch.randn(*s, dtype=torch.bfloat16) * 0.2)(D, ks) * 0.5),
            "ssm_k": _w((lambda *s: torch.randn(*s, dtype=torch.bfloat16) * 0.4)(D, S) * (1.0 / S ** 0.5)),
            "ssm_D": _w((lambda *s: torch.randn(*s, dtype=torch.bfloat16) * 0.2)(D)),
        }
    _A_in = _A_setup()

    def _B_setup():
        B, S, D, F = 1, 2048, 1024, 3584
        return {
            "x": _w((lambda *s: torch.randn(*s, dtype=torch.bfloat16) * 0.05)(B, S, D)),
            "w1": _w((lambda *s: torch.randn(*s, dtype=torch.bfloat16) * 0.8)(D, F) * (D ** -0.5)),
            "w2": _w((lambda *s: torch.randn(*s, dtype=torch.bfloat16) * 0.1)(F, D) * (F ** -0.5)),
        }
    _B_in = _B_setup()

    return {
        "A_q": _A_in["q"],
        "A_k": _A_in["k"],
        "A_v": _A_in["v"],
        "A_shift_k": _A_in["shift_k"],
        "A_ssm_k": _A_in["ssm_k"],
        "A_ssm_D": _A_in["ssm_D"],
        "B_x": _B_in["x"],
        "B_w1": _B_in["w1"],
        "B_w2": _B_in["w2"],
    }

def reference(A_q, A_k, A_v, A_shift_k, A_ssm_k, A_ssm_D, B_x, B_w1, B_w2):
    def _A_ref(q, k, v, shift_k, ssm_k, ssm_D):
        B, S, D = q.shape
        ks = shift_k.shape[-1]
        k_pad = _pad(k, ((0, 0), (ks - 1, 0), (0, 0)))
        k_shift = torch.zeros_like(k)
        for t in range(ks):
            k_shift = k_shift + k_pad[:, t:t + S, :] * shift_k[:, ks - 1 - t][None, None, :]
        kv = k_shift * v
        L = 2 * S
        kv_pad = _pad(kv, ((0, 0), (0, S), (0, 0)))
        kf_pad = _pad(ssm_k, ((0, 0), (0, S)))
        kv_f = (lambda x, axes=(-2,-1): torch.fft.rfft2(x.float(), dim=axes))(kv_pad[:, None, :, :], axes=(1, 2))
        kf_f = (lambda x, axes=(-2,-1): torch.fft.rfft2(x.float(), dim=axes))(kf_pad[:, None, :], axes=(1, 2))
        kf_f = (lambda x, *d: x.permute(*d))(kf_f, 1, 2, 0)
        y_f = kv_f * kf_f[None, :, :, :]
        y = (lambda x, s, axes=(-2,-1): torch.fft.irfft2(x, s=s, dim=axes).to(torch.bfloat16))(y_f, (1, L), axes=(1, 2))[:, 0, :S, :]
        y = y + kv * ssm_D[None, None, :]
        return q * y

    def _B_ref(x, w1, w2):
        h = torch.einsum("bsd,df->bsf", x, w1)
        h = _silu(h)
        out = torch.einsum("bsf,fd->bsd", h, w2)
        return out

    x = A_q
    xn = _layernorm(x, axis=-1)
    return x + _A_ref(xn, A_k, A_v, A_shift_k, A_ssm_k, A_ssm_D) + _B_ref(xn, B_w1, B_w2)
```

```cpp
#include <hip/hip_runtime.h>
#include <hip/hip_bf16.h>
#include <stdint.h>
typedef unsigned short u16;
constexpr int S=2048, D=1024, F=3584;
__device__ __forceinline__ float bf2f(u16 x){ return __uint_as_float(((unsigned)x)<<16); }
__device__ __forceinline__ u16 f2bf(float f){ __bf16 b=(__bf16)f; return __builtin_bit_cast(u16,b); }
__device__ __forceinline__ float rbf(float f){ return bf2f(f2bf(f)); }

__global__ void k_ln(const u16* q, u16* xn){
  int r=blockIdx.x; __shared__ float red[256];
  float v[4]; float s=0;
  for(int i=0;i<4;i++){ v[i]=bf2f(q[r*D+threadIdx.x*4+i]); s+=v[i]; }
  red[threadIdx.x]=s; __syncthreads();
  for(int o=128;o>0;o>>=1){ if(threadIdx.x<o) red[threadIdx.x]+=red[threadIdx.x+o]; __syncthreads(); }
  float mean=red[0]/D; __syncthreads();
  float s2=0; for(int i=0;i<4;i++){ float d=v[i]-mean; s2+=d*d; }
  red[threadIdx.x]=s2; __syncthreads();
  for(int o=128;o>0;o>>=1){ if(threadIdx.x<o) red[threadIdx.x]+=red[threadIdx.x+o]; __syncthreads(); }
  float var=red[0]/D;
  float m=rbf(mean), vv=rbf(var); float den=rbf(sqrtf(rbf(vv+1e-5f)));
  for(int i=0;i<4;i++) xn[r*D+threadIdx.x*4+i]=f2bf(rbf(v[i]-m)/den);
}
__global__ void k_kv(const u16* k,const u16* v,const u16* sk,u16* kvT){
  int idx=blockIdx.x*256+threadIdx.x; int s=idx/D, d=idx%D;
  float acc=0;
  for(int t=0;t<4;t++){ int ss=s-3+t; float kk= ss>=0? bf2f(k[ss*D+d]):0.f; acc=rbf(acc+rbf(kk*bf2f(sk[d*4+3-t]))); }
  kvT[d*S+s]=f2bf(acc*bf2f(v[idx]));
}
__global__ void k_conv(const u16* kvT,const u16* h,const u16* Dv,u16* yT){
  int d=blockIdx.x; __shared__ float su[S], sh[S];
  for(int i=threadIdx.x;i<S;i+=256){ su[i]=bf2f(kvT[d*S+i]); sh[i]=bf2f(h[d*S+i]); }
  __syncthreads();
  for(int s=threadIdx.x;s<S;s+=256){ float a=0; for(int j=0;j<=s;j++) a+=sh[j]*su[s-j];
    yT[d*S+s]=f2bf(rbf(a)+rbf(su[s]*bf2f(Dv[d]))); }
}
template<int MODE> __global__ void k_gemm(const u16* A,const u16* B,u16* C,int M,int N,int K,const u16* x,const u16* xn,const u16* yT){
  __shared__ float sa[32][65], sb[32][65];
  int tx=threadIdx.x%16, ty=threadIdx.x/16; int m0=blockIdx.y*64, n0=blockIdx.x*64;
  float acc[4][4]={};
  for(int k0=0;k0<K;k0+=32){
    for(int i=threadIdx.x;i<64*32;i+=256){ int r=i/32,c=i%32; sa[c][r]=bf2f(A[(m0+r)*K+k0+c]); int kk=i/64,n=i%64; sb[kk][n]=bf2f(B[(k0+kk)*N+n0+n]); }
    __syncthreads();
    for(int kk=0;kk<32;kk++){ float a[4],b[4]; for(int i=0;i<4;i++){a[i]=sa[kk][ty*4+i]; b[i]=sb[kk][tx*4+i];}
      for(int i=0;i<4;i++)for(int j=0;j<4;j++) acc[i][j]+=a[i]*b[j]; }
    __syncthreads();
  }
  for(int i=0;i<4;i++)for(int j=0;j<4;j++){ int m=m0+ty*4+i,n=n0+tx*4+j; float v=rbf(acc[i][j]);
    if(MODE==0){ v=v/(1.f+__expf(-v)); C[m*N+n]=f2bf(v);} else { float a=rbf(bf2f(xn[m*N+n])*bf2f(yT[n*S+m])); C[m*N+n]=f2bf(rbf(bf2f(x[m*N+n])+a)+v);} }
}
extern "C" void kernel_launch(void* const* d_in, const int* in_sizes, int n_in, void* d_out, int out_size, void* d_ws, size_t ws_size, hipStream_t stream){
  const u16* q=(const u16*)d_in[0]; const u16* k=(const u16*)d_in[1]; const u16* v=(const u16*)d_in[2];
  const u16* sk=(const u16*)d_in[3]; const u16* h=(const u16*)d_in[4]; const u16* Dv=(const u16*)d_in[5];
  const u16* w1=(const u16*)d_in[7]; const u16* w2=(const u16*)d_in[8];
  u16* ws=(u16*)d_ws; u16* xn=ws; u16* kvT=ws+S*D; u16* yT=kvT+S*D; u16* H=yT+S*D;
  k_ln<<<S,256,0,stream>>>(q,xn);
  k_kv<<<S*D/256,256,0,stream>>>(k,v,sk,kvT);
  k_conv<<<D,256,0,stream>>>(kvT,h,Dv,yT);
  k_gemm<0><<<dim3(F/64,S/64),256,0,stream>>>(xn,w1,H,S,F,D,nullptr,nullptr,nullptr);
  k_gemm<1><<<dim3(D/64,S/64),256,0,stream>>>(H,w2,(u16*)d_out,S,D,F,q,xn,yT);
}
```

```cpp
#include <hip/hip_runtime.h>

__global__ __launch_bounds__(512, 2) void fused_kernel(const void* q, const void* k, const void* v, const void* shift_k, const void* ssm_k,
                                                       const void* ssm_d, const void* w1, const void* w2, void* out, void* ws) {
  asm volatile(
      ".Lk_start:\n"
      "s_load_dwordx16 s[4:19], s[0:1], 0\n"
      "s_load_dwordx4 s[20:23], s[0:1], 64\n"
      "s_getpc_b64 s[50:51]\n"
      ".Lk_pc0:\n"
      "s_and_b32 s52, s2, 0xf8\n"
      "v_lshrrev_b32 v1, 6, v0\n"
      "v_and_b32 v2, 1, v0\n"
      "v_lshl_or_b32 v2, v2, 8, v1\n"
      "v_add_lshl_u32 v2, v2, s52, 7\n"
      "v_min_u32 v2, .Lk_end-.Lk_pc0-4, v2\n"
      "s_mov_b64 exec, 3\n"
      "global_load_dword v255, v2, s[50:51]\n"
      "s_mov_b64 exec, -1\n"
      "v_lshrrev_b32 v1, 6, v0\n"
      "v_and_b32 v250, 63, v0\n"
      "s_mov_b32 s30, s2\n"
      "v_readfirstlane_b32 s29, v1\n"
      "v_and_b32 v251, 15, v0\n"
      "v_lshrrev_b32 v252, 4, v250\n"
      "v_lshlrev_b32 v253, 4, v250\n"
      "s_lshr_b32 s31, s2, 3\n"
      "s_bfe_u32 s50, s2, 0x10002\n"
      "s_lshl_b32 s50, s50, 3\n"
      "s_lshr_b32 s51, s31, 2\n"
      "s_add_u32 s24, s50, s51\n"
      "s_and_b32 s50, s2, 3\n"
      "s_lshl_b32 s50, s50, 2\n"
      "s_and_b32 s51, s31, 3\n"
      "s_add_u32 s25, s50, s51\n"
      "s_lshl_b32 s26, s24, 4\n"
      "s_add_u32 s26, s26, s25\n"
      "s_lshr_b32 s27, s25, 3\n"
      "s_and_b32 s28, s25, 7\n"
      "s_cmp_lt_u32 s29, 4\n"
      "s_cbranch_scc1 .Lk_A\n"
      "s_setprio 3\n"
      "v_bfe_u32 v1, v0, 3, 2\n"
      "v_bfe_u32 v2, v0, 5, 1\n"
      "v_lshl_or_b32 v1, v1, 1, v2\n"
      "v_and_b32 v9, 7, v0\n"
      "v_xor_b32 v2, v9, v252\n"
      "v_lshlrev_b32 v2, 4, v2\n"
      "v_lshl_or_b32 v10, v1, 11, v2\n"
      "v_xor_b32 v11, 64, v10\n"
      "v_add_u32 v11, 15360, v11\n"
      "v_add_u32 v12, 30720, v10\n"
      "v_add_u32 v13, 30720, v11\n"
      "s_and_b32 s73, s29, 3\n"
      "s_add_u32 s59, s24, s25\n"
      "s_and_b32 s59, s59, 3\n"
      "s_xor_b32 s55, s24, s25\n"
      "s_and_b32 s55, s55, 12\n"
      "s_or_b32 s59, s59, s55\n"
      "s_lshl_b32 s62, s24, 18\n"
      "s_lshl_b32 s53, s73, 16\n"
      "s_add_u32 s62, s62, s53\n"
      "s_and_b32 s58, s59, 14\n"
      "s_lshl_b32 s60, s58, 7\n"
      "s_add_u32 s60, s60, s62\n"
      "s_lshl_b32 s65, s73, 12\n"
      "s_mov_b32 s38, 4194304\n"
      "s_mov_b32 s39, 0x20000\n"
      "s_mul_i32 s63, s25, 448\n"
      "s_mul_i32 s53, s73, 114688\n"
      "s_add_u32 s63, s63, s53\n"
      "s_mul_i32 s61, s58, 458752\n"
      "s_add_u32 s61, s61, s63\n"
      "s_mul_i32 s66, s73, 7168\n"
      "s_add_u32 s66, s66, 16384\n"
      "s_mov_b32 s42, 7340032\n"
      "s_mov_b32 s43, 0x20000\n"
      "v_lshrrev_b32 v1, 2, v250\n"
      "v_and_b32 v2, 48, v253\n"
      "s_movk_i32 s50, 147\n"
      "s_bitcmp1_b32 s25, 0\n"
      "s_movk_i32 s52, 6720\n"
      "s_cselect_b32 s51, 7616, s52\n"
      "s_cselect_b32 s53, 0xffffffc0, 64\n"
      "s_cselect_b32 s54, 384, 0\n"
      "v_add_u32 v3, 0, v1\n"
      "v_mul_u32_u24 v4, s50, v3\n"
      "v_lshrrev_b32 v4, 10, v4\n"
      "v_lshlrev_b32 v5, 2, v4\n"
      "v_and_b32 v5, 32, v5\n"
      "v_xad_u32 v5, v5, v2, s54\n"
      "v_mad_i32_i24 v5, v3, s53, v5\n"
      "v_mad_i32_i24 v14, v4, s51, v5\n"
      "v_add_u32 v3, 16, v1\n"
      "v_mul_u32_u24 v4, s50, v3\n"
      "v_lshrrev_b32 v4, 10, v4\n"
      "v_lshlrev_b32 v5, 2, v4\n"
      "v_and_b32 v5, 32, v5\n"
      "v_xad_u32 v5, v5, v2, s54\n"
      "v_mad_i32_i24 v5, v3, s53, v5\n"
      "v_mad_i32_i24 v15, v4, s51, v5\n"
      "v_subrev_u32 v15, 1024, v15\n"
      "s_waitcnt lgkmcnt(0)\n"
      "s_mov_b64 s[36:37], s[4:5]\n"
      "s_mov_b64 s[40:41], s[16:17]\n"
      "s_mov_b64 s[32:33], s[22:23]\n"
      "s_add_u32 m0, s65, 45056\n"
      "s_nop 0\n"
      "buffer_load_dwordx4 v10, s[36:39], s60 offen lds\n"
      "buffer_load_dwordx4 v11, s[36:39], s60 offen offset:1024 lds\n"
      "buffer_load_dwordx4 v12, s[36:39], s60 offen offset:2048 lds\n"
      "buffer_load_dwordx4 v13, s[36:39], s60 offen offset:3072 lds\n"
      "s_add_u32 m0, s66, 45056\n"
      "s_nop 0\n"
      "buffer_load_dwordx4 v14, s[40:43], s61 offen lds\n"
      "buffer_load_dwordx4 v15, s[40:43], s61 offen offset:1024 lds\n"
      "v_add_u32 v3, 32, v1\n"
      "v_mul_u32_u24 v4, s50, v3\n"
      "v_lshrrev_b32 v4, 10, v4\n"
      "v_lshlrev_b32 v5, 2, v4\n"
      "v_and_b32 v5, 32, v5\n"
      "v_xad_u32 v5, v5, v2, s54\n"
      "v_mad_i32_i24 v5, v3, s53, v5\n"
      "v_mad_i32_i24 v16, v4, s51, v5\n"
      "v_subrev_u32 v16, 2048, v16\n"
      "buffer_load_dwordx4 v16, s[40:43], s61 offen offset:2048 lds\n"
      "v_add_u32 v3, 48, v1\n"
      "v_mul_u32_u24 v4, s50, v3\n"
      "v_lshrrev_b32 v4, 10, v4\n"
      "v_lshlrev_b32 v5, 2, v4\n"
      "v_and_b32 v5, 32, v5\n"
      "v_xad_u32 v5, v5, v2, s54\n"
      "v_mad_i32_i24 v5, v3, s53, v5\n"
      "v_mad_i32_i24 v17, v4, s51, v5\n"
      "v_subrev_u32 v17, 3072, v17\n"
      "buffer_load_dwordx4 v17, s[40:43], s61 offen offset:3072 lds\n"
      "v_add_u32 v3, 64, v1\n"
      "v_mul_u32_u24 v4, s50, v3\n"
      "v_lshrrev_b32 v4, 10, v4\n"
      "v_lshlrev_b32 v5, 2, v4\n"
      "v_and_b32 v5, 32, v5\n"
      "v_xad_u32 v5, v5, v2, s54\n"
      "v_mad_i32_i24 v5, v3, s53, v5\n"
      "v_mad_i32_i24 v18, v4, s51, v5\n"
      "v_add_u32 v3, 80, v1\n"
      "v_mul_u32_u24 v4, s50, v3\n"
      "v_lshrrev_b32 v4, 10, v4\n"
      "v_lshlrev_b32 v5, 2, v4\n"
      "v_and_b32 v5, 32, v5\n"
      "v_xad_u32 v5, v5, v2, s54\n"
      "v_mad_i32_i24 v5, v3, s53, v5\n"
      "v_mad_i32_i24 v19, v4, s51, v5\n"
      "v_subrev_u32 v19, 1024, v19\n"
      "v_add_u32 v3, 96, v1\n"
      "v_mul_u32_u24 v4, s50, v3\n"
      "v_lshrrev_b32 v4, 10, v4\n"
      "v_lshlrev_b32 v5, 2, v4\n"
      "v_and_b32 v5, 32, v5\n"
      "v_xad_u32 v5, v5, v2, s54\n"
      "v_mad_i32_i24 v5, v3, s53, v5\n"
      "v_mad_i32_i24 v20, v4, s51, v5\n"
      "v_subrev_u32 v20, 2048, v20\n"
      "s_lshl_b32 s51, s25, 7\n"
      "s_add_u32 s67, s62, s51\n"
      "s_and_b32 s50, s24, 3\n"
      "s_mov_b32 s52, 1703093264\n"
      "s_mov_b32 s53, 4275808711\n"
      "s_cmp_eq_u32 s50, 1\n"
      "s_cselect_b32 s52, 1750286864, s52\n"
      "s_cselect_b32 s53, 4275809431, s53\n"
      "s_cmp_eq_u32 s50, 2\n"
      "s_cselect_b32 s52, 2253603344, s52\n"
      "s_cselect_b32 s53, 4275817111, s53\n"
      "s_cmp_eq_u32 s50, 3\n"
      "s_cselect_b32 s52, 1985229328, s52\n"
      "s_cselect_b32 s53, 4275878552, s53\n"
      "v_lshlrev_b32 v1, 2, v251\n"
      "v_lshrrev_b64 v[2:3], v1, s[52:53]\n"
      "v_and_b32 v4, 12, v2\n"
      "v_cmp_eq_u32 vcc, 0, v4\n"
      "v_add_u32 v3, s59, v2\n"
      "v_add_u32 v4, s58, v2\n"
      "v_cndmask_b32 v3, v3, v4, vcc\n"
      "v_xor_b32 v2, s59, v2\n"
      "v_and_b32 v2, 12, v2\n"
      "v_and_or_b32 v2, v3, 3, v2\n"
      "v_lshl_add_u32 v147, v2, 7, s62\n"
      "v_mul_u32_u24 v148, 458752, v2\n"
      "v_add_u32 v148, s63, v148\n"
      "v_readlane_b32 s62, v147, 1\n"
      "v_readlane_b32 s63, v148, 1\n"
      "s_lshl_b32 s50, s28, 1\n"
      "s_and_b32 s51, s73, 1\n"
      "s_add_u32 s50, s50, s51\n"
      "v_cmp_eq_u32 vcc, s50, v2\n"
      "s_lshl_b32 s50, s27, 13\n"
      "s_and_b32 s51, s73, 2\n"
      "s_lshl_b32 s51, s51, 11\n"
      "s_add_u32 s50, s50, s51\n"
      "s_ff1_i32_b32 s64, vcc_lo\n"
      "v_lshrrev_b32 v149, 1, v251\n"
      "v_xor_b32 v149, v149, v252\n"
      "v_lshlrev_b32 v149, 4, v149\n"
      "v_lshl_or_b32 v149, v251, 7, v149\n"
      "v_add_u32 v149, s50, v149\n"
      "s_load_dwordx2 s[48:49], s[22:23], 0\n"
      "s_lshl_b32 s74, s25, 6\n"
      "s_lshl_b32 s50, s24, 2\n"
      "s_add_u32 s50, s50, s74\n"
      "s_and_b32 s51, s73, 2\n"
      "s_add_u32 s51, s51, s50\n"
      "s_lshl_b32 s51, s51, 1\n"
      "s_load_dword s76, s[14:15], s51\n"
      "s_and_b32 s52, s73, 1\n"
      "s_lshl_b32 s52, s52, 5\n"
      "s_add_u32 s52, s52, s74\n"
      "s_add_u32 s74, s50, s73\n"
      "v_lshrrev_b32 v8, 3, v250\n"
      "v_lshlrev_b32 v1, 5, v8\n"
      "s_lshl_b32 s53, s52, 3\n"
      "v_add_u32 v1, s53, v1\n"
      "global_load_dwordx4 v[32:35], v1, s[10:11]\n"
      "global_load_dwordx4 v[36:39], v1, s[10:11] offset:16\n"
      "s_mov_b32 s32, s22\n"
      "s_and_b32 s33, s23, 0xffff\n"
      "s_mov_b32 s34, 41943040\n"
      "s_mov_b32 s35, 0x20000\n"
      "s_mov_b32 s84, s6\n"
      "s_and_b32 s85, s7, 0xffff\n"
      "s_mov_b32 s86, 4194304\n"
      "s_mov_b32 s87, 0x20000\n"
      "s_mov_b32 s44, s8\n"
      "s_and_b32 s45, s9, 0xffff\n"
      "s_mov_b32 s46, 4194304\n"
      "s_mov_b32 s47, 0x20000\n"
      "s_lshl_b32 s50, s73, 2\n"
      "s_add_u32 m0, s66, 49152\n"
      "s_nop 0\n"
      "buffer_load_dwordx4 v18, s[40:43], s61 offen lds\n"
      "s_add_u32 s50, s50, 1\n"
      "v_lshlrev_b32 v25, 4, v9\n"
      "v_lshl_or_b32 v28, v8, 14, v25\n"
      "v_lshl_or_b32 v25, v8, 11, v25\n"
      "s_add_u32 s51, s50, 0\n"
      "s_and_b32 s51, s51, 7\n"
      "s_lshl_b32 s51, s51, 4\n"
      "v_xor_b32 v21, s51, v25\n"
      "s_add_u32 s51, s50, 1\n"
      "buffer_load_dwordx4 v19, s[40:43], s61 offen offset:1024 lds\n"
      "s_and_b32 s51, s51, 7\n"
      "s_lshl_b32 s51, s51, 4\n"
      "v_xor_b32 v22, s51, v25\n"
      "v_add_u32 v22, 15360, v22\n"
      "s_add_u32 s51, s50, 2\n"
      "s_and_b32 s51, s51, 7\n"
      "s_lshl_b32 s51, s51, 4\n"
      "v_xor_b32 v23, s51, v25\n"
      "v_add_u32 v23, 30720, v23\n"
      "s_add_u32 s51, s50, 3\n"
      "buffer_load_dwordx4 v20, s[40:43], s61 offen offset:2048 lds\n"
      "s_and_b32 s51, s51, 7\n"
      "s_lshl_b32 s51, s51, 4\n"
      "v_xor_b32 v24, s51, v25\n"
      "v_add_u32 v24, 46080, v24\n"
      "s_lshl_b32 s51, s25, 7\n"
      "s_lshl_b32 s68, s24, 18\n"
      "s_add_u32 s68, s68, s51\n"
      "s_lshl_b32 s53, s73, 12\n"
      "s_add_u32 s68, s68, s53\n"
      "s_add_u32 m0, s66, 90112\n"
      "s_nop 0\n"
      "buffer_load_dwordx4 v14, s[40:43], s63 offen lds\n"
      "s_sub_u32 s68, s68, 16384\n"
      "s_cmp_eq_u32 s24, 0\n"
      "s_cselect_b32 s68, 0x7fff0000, s68\n"
      "s_add_u32 s69, s53, 136192\n"
      "s_lshr_b32 s54, s73, 1\n"
      "s_lshl_b32 s55, s54, 3\n"
      "s_add_u32 s55, s55, 8\n"
      "v_sub_u32 v1, s55, v9\n"
      "v_subrev_u32 v2, 1, v1\n"
      "s_and_b32 s55, s73, 1\n"
      "s_lshl_b32 s55, s55, 2\n"
      "buffer_load_dwordx4 v15, s[40:43], s63 offen offset:1024 lds\n"
      "v_add_u32 v3, s55, v252\n"
      "v_and_b32 v4, 1, v8\n"
      "v_lshlrev_b32 v4, 3, v4\n"
      "v_add_u32 v4, 135168, v4\n"
      "v_xor_b32 v5, v3, v1\n"
      "v_and_b32 v5, 7, v5\n"
      "v_lshl_add_u32 v5, v5, 4, v4\n"
      "v_lshl_add_u32 v26, v1, 10, v5\n"
      "v_xor_b32 v5, v3, v2\n"
      "buffer_load_dwordx4 v16, s[40:43], s63 offen offset:2048 lds\n"
      "v_and_b32 v5, 7, v5\n"
      "v_lshl_add_u32 v5, v5, 4, v4\n"
      "v_lshl_add_u32 v27, v2, 10, v5\n"
      "s_lshl_b32 s70, s52, 12\n"
      "s_lshl_b32 s51, s24, 8\n"
      "s_lshl_b32 s54, s54, 7\n"
      "s_add_u32 s51, s51, s54\n"
      "s_sub_u32 s51, 8392576, s51\n"
      "s_add_u32 s70, s70, s51\n"
      "buffer_load_dwordx4 v17, s[40:43], s63 offen offset:3072 lds\n"
      "v_mov_b32 v236, 0xffff0000\n"
      "s_mul_i32 s75, s73, 13344\n"
      "s_add_u32 s75, s75, 90112\n"
      "s_mul_i32 s82, s73, 4160\n"
      "s_add_u32 s82, s82, 143712\n"
      "s_lshl_b32 s50, s26, 3\n"
      "s_lshl_b32 s51, s25, 7\n"
      "s_lshl_b32 s53, s24, 3\n"
      "s_add_u32 s51, s51, s53\n"
      "s_add_u32 s51, s51, 2048\n"
      "s_add_u32 m0, s66, 94208\n"
      "s_nop 0\n"
      "buffer_load_dwordx4 v18, s[40:43], s63 offen lds\n"
      "s_sub_u32 s51, s51, s50\n"
      "v_mov_b32 v144, s50\n"
      "v_mad_u32_u24 v144, v250, s51, v144\n"
      "s_cmp_eq_u32 s73, 0\n"
      "s_cselect_b32 s71, 0, 0x7fff0000\n"
      "s_waitcnt lgkmcnt(0)\n"
      "v_bfe_u32 v206, v250, 2, 2\n"
      "v_xor_b32 v206, v206, v252\n"
      "v_and_b32 v207, 48, v253\n"
      "v_lshl_or_b32 v206, v206, 6, v207\n"
      "buffer_load_dwordx4 v19, s[40:43], s63 offen offset:1024 lds\n"
      "v_lshl_or_b32 v206, v252, 11, v206\n"
      "s_lshl_b32 s50, s73, 15\n"
      "s_mul_i32 s51, s27, 3670016\n"
      "s_add_u32 s50, s50, s51\n"
      "s_lshl_b32 s51, s28, 8\n"
      "s_add_u32 s50, s50, s51\n"
      "v_add_u32 v200, s50, v206\n"
      "v_add_u32 v201, 7160, v200\n"
      "v_add_u32 v202, 14320, v200\n"
      "buffer_load_dwordx4 v20, s[40:43], s63 offen offset:2048 lds\n"
      "v_add_u32 v203, 21480, v200\n"
      "s_mul_i32 s83, s73, 4128\n"
      "v_and_b32 v172, 1, v252\n"
      "v_lshlrev_b32 v172, 5, v172\n"
      "v_xor_b32 v172, v172, v253\n"
      "v_add_u32 v173, 16, v172\n"
      "v_lshrrev_b32 v174, 3, v173\n"
      "v_and_b32 v174, 32, v174\n"
      "v_xor_b32 v173, v173, v174\n"
      "v_add_u32 v173, s75, v173\n"
      "s_add_u32 m0, s65, 90112\n"
      "s_nop 0\n"
      "buffer_load_dwordx4 v10, s[36:39], s62 offen lds\n"
      "v_add_u32 v174, s75, v253\n"
      "v_and_b32 v204, 1, v250\n"
      "v_cmp_eq_u32 vcc, 1, v204\n"
      "s_add_u32 s50, s75, 9176\n"
      "s_sub_u32 s51, s82, 60\n"
      "v_mov_b32 v205, s50\n"
      "v_mov_b32 v206, s51\n"
      "v_cndmask_b32 v160, v205, v206, vcc\n"
      "v_mov_b32 v205, 4624\n"
      "buffer_load_dwordx4 v11, s[36:39], s62 offen offset:1024 lds\n"
      "v_readlane_b32 s60, v147, 2\n"
      "v_readlane_b32 s61, v148, 2\n"
      "v_cndmask_b32 v162, v205, 0, vcc\n"
      "v_lshl_add_u32 v160, v251, 2, v160\n"
      "v_lshl_add_u32 v160, v252, 3, v160\n"
      "v_add_u32 v162, s75, v162\n"
      "v_lshrrev_b32 v205, 1, v251\n"
      "v_lshlrev_b32 v205, 6, v205\n"
      "v_sub_u32 v161, 448, v205\n"
      "v_lshl_add_u32 v161, v252, 3, v161\n"
      "v_lshlrev_b32 v206, 1, v251\n"
      "v_lshl_add_u32 v206, v252, 2, v206\n"
      "buffer_load_dwordx4 v12, s[36:39], s62 offen offset:2048 lds\n"
      "s_and_b32 s50, s74, 1\n"
      "s_lshl_b32 s50, s50, 4\n"
      "s_lshr_b32 s76, s76, s50\n"
      "s_and_b32 s76, s76, 0xffff\n"
      "v_mov_b32 v207, s76\n"
      "v_cmp_eq_u32 vcc, 30, v206\n"
      "s_nop 1\n"
      "v_cndmask_b32 v168, 0, v207, vcc\n"
      "v_cmp_eq_u32 vcc, 28, v206\n"
      "s_nop 1\n"
      "v_cndmask_b32 v169, 0, v207, vcc\n"
      "buffer_load_dwordx4 v13, s[36:39], s62 offen offset:3072 lds\n"
      "s_waitcnt vmcnt(11)\n"
      "s_barrier\n"
      "s_add_u32 m0, s66, 0\n"
      "s_nop 0\n"
      "buffer_load_dwordx4 v14, s[40:43], s61 offen lds\n"
      "v_cmp_eq_u32 vcc, 14, v206\n"
      "s_nop 1\n"
      "v_cndmask_b32 v170, 0, v207, vcc\n"
      "v_cmp_eq_u32 vcc, 12, v206\n"
      "s_nop 1\n"
      "v_cndmask_b32 v171, 0, v207, vcc\n"
      "v_lshl_add_u32 v175, v250, 3, s75\n"
      "s_sub_u32 s50, s82, 64\n"
      "s_add_u32 s51, s75, 9172\n"
      "v_lshl_add_u32 v166, v251, 2, s50\n"
      "v_lshl_add_u32 v167, v251, 2, s51\n"
      "v_cmp_gt_u32 vcc, 32, v250\n"
      "buffer_load_dwordx4 v15, s[40:43], s61 offen offset:1024 lds\n"
      "v_mov_b32 v218, 0\n"
      "v_mov_b32 v219, 0\n"
      "v_cndmask_b32 v166, v166, v167, vcc\n"
      "s_lshr_b32 s57, s73, 1\n"
      "s_and_b32 s58, s73, 1\n"
      "s_lshl_b32 s50, s27, 12\n"
      "s_lshl_b32 s51, s57, 11\n"
      "s_add_u32 s52, s50, s51\n"
      "s_xor_b32 s53, s52, 4096\n"
      "v_add_u32 v192, s52, v253\n"
      "buffer_load_dwordx4 v16, s[40:43], s61 offen offset:2048 lds\n"
      "v_add_u32 v193, s53, v253\n"
      "v_lshrrev_b32 v196, 2, v251\n"
      "s_lshl_b32 s50, s58, 1\n"
      "v_xor_b32 v197, s50, v196\n"
      "v_and_b32 v198, 48, v253\n"
      "v_lshl_or_b32 v197, v197, 6, v198\n"
      "v_lshl_or_b32 v197, v196, 8, v197\n"
      "v_xor_b32 v198, 64, v197\n"
      "s_movk_i32 s50, 1032\n"
      "v_mad_u32_u24 v194, v252, s50, v197\n"
      "v_mad_u32_u24 v195, v252, s50, v198\n"
      "buffer_load_dwordx4 v17, s[40:43], s61 offen offset:3072 lds\n"
      "v_mov_b32 v199, -2\n"
      "s_lshl_b32 s52, s57, 5\n"
      "v_add_u32 v208, s52, v251\n"
      "s_lshl_b32 s53, s27, 1\n"
      "s_add_u32 s53, s53, s57\n"
      "s_lshl_b32 s53, s53, 9\n"
      "s_add_u32 s53, s53, 160768\n"
      "v_lshl_add_u32 v211, v251, 4, s53\n"
      "s_lshl_b32 s53, s58, 3\n"
      "v_add_u32 v209, s53, v252\n"
      "s_add_u32 m0, s66, 4096\n"
      "s_nop 0\n"
      "buffer_load_dwordx4 v18, s[40:43], s61 offen lds\n"
      "v_xor_b32 v209, v209, v251\n"
      "v_lshlrev_b32 v209, 4, v209\n"
      "v_lshl_add_u32 v208, v208, 8, v209\n"
      "v_add_u32 v208, 118784, v208\n"
      "v_xor_b32 v209, 64, v208\n"
      "v_lshl_add_u32 v210, v252, 3, v196\n"
      "s_lshl_b32 s53, s58, 6\n"
      "v_add_u32 v210, s53, v210\n"
      "v_bfe_u32 v212, v250, 1, 1\n"
      "v_lshlrev_b32 v212, 6, v212\n"
      "buffer_load_dwordx4 v19, s[40:43], s61 offen offset:1024 lds\n"
      "v_lshl_add_u32 v210, v210, 7, v212\n"
      "v_lshl_add_u32 v210, v204, 4, v210\n"
      "s_lshl_b32 s53, s57, 5\n"
      "s_add_u32 s53, s53, 102400\n"
      "v_add_u32 v210, s53, v210\n"
      "s_lshl_b32 s52, s57, 5\n"
      "v_lshrrev_b32 v212, 3, v250\n"
      "v_and_b32 v213, 7, v250\n"
      "s_lshl_b32 s53, s58, 3\n"
      "v_add_u32 v214, s53, v213\n"
      "buffer_load_dwordx4 v20, s[40:43], s61 offen offset:2048 lds\n"
      "v_xor_b32 v214, v214, v212\n"
      "v_lshlrev_b32 v214, 4, v214\n"
      "v_add_u32 v215, s52, v212\n"
      "v_lshl_add_u32 v214, v215, 8, v214\n"
      "v_add_u32 v214, 118784, v214\n"
      "v_xor_b32 v215, 128, v214\n"
      "v_and_b32 v216, 3, v212\n"
      "v_lshrrev_b32 v212, 2, v212\n"
      "v_lshl_or_b32 v216, v216, 1, v212\n"
      "v_lshlrev_b32 v213, 4, v213\n"
      "s_add_u32 m0, s65, 0\n"
      "s_nop 0\n"
      "buffer_load_dwordx4 v10, s[36:39], s60 offen lds\n"
      "v_lshl_or_b32 v216, v216, 11, v213\n"
      "s_lshl_b32 s53, s24, 7\n"
      "s_lshl_b32 s54, s27, 6\n"
      "s_add_u32 s53, s53, s54\n"
      "s_add_u32 s53, s53, s52\n"
      "s_lshl_b32 s53, s53, 11\n"
      "s_lshl_b32 s54, s28, 8\n"
      "s_add_u32 s53, s53, s54\n"
      "s_lshl_b32 s54, s58, 7\n"
      "s_add_u32 s3, s53, s54\n"
      "v_mov_b32 v217, 0xffff0000\n"
      "buffer_load_dwordx4 v11, s[36:39], s60 offen offset:1024 lds\n"
      "v_readlane_b32 s62, v147, 3\n"
      "v_readlane_b32 s63, v148, 3\n"
      "v_and_b32 v212, 48, v253\n"
      "v_lshl_or_b32 v212, v196, 7, v212\n"
      "v_and_b32 v213, 1, v252\n"
      "v_lshl_or_b32 v212, v213, 6, v212\n"
      "v_lshrrev_b32 v213, 5, v250\n"
      "v_lshl_or_b32 v212, v213, 9, v212\n"
      "s_mul_i32 s50, s48, 0x9e3779b9\n"
      "s_mul_hi_u32 s51, s48, 0x7f4a7c15\n"
      "s_mul_i32 s52, s49, 0x7f4a7c15\n"
      "s_add_u32 s50, s50, s51\n"
      "buffer_load_dwordx4 v12, s[36:39], s60 offen offset:2048 lds\n"
      "s_add_u32 s50, s50, s52\n"
      "s_mul_i32 s48, s48, 0x7f4a7c15\n"
      "s_add_u32 s48, s48, 1\n"
      "s_addc_u32 s49, s50, 0\n"
      "v_mov_b32 v248, s48\n"
      "v_mov_b32 v249, s49\n"
      "s_lshl_b32 s50, s24, 7\n"
      "s_lshl_b32 s51, s25, 7\n"
      "s_add_u32 s51, s51, 2048\n"
      "s_sub_u32 s51, s51, s50\n"
      "s_add_u32 s50, s50, s71\n"
      "buffer_load_dwordx4 v13, s[36:39], s60 offen offset:3072 lds\n"
      "s_add_u32 s50, s50, 4096\n"
      "v_and_b32 v29, 1, v252\n"
      "v_mul_u32_u24 v29, s51, v29\n"
      "v_lshl_add_u32 v29, v251, 3, v29\n"
      "v_add_u32 v29, s50, v29\n"
      "v_lshrrev_b32 v1, 2, v250\n"
      "v_and_b32 v2, 48, v253\n"
      "s_lshl_b32 s50, s24, 15\n"
      "s_lshl_b32 s51, s25, 6\n"
      "s_add_u32 s50, s50, s51\n"
      "s_mov_b32 m0, s69\n"
      "s_nop 0\n"
      "buffer_load_dwordx4 v21, s[84:87], s67 offen lds\n"
      "s_add_u32 s50, s50, 1024\n"
      "s_lshl_b32 s51, s25, 11\n"
      "s_lshl_b32 s52, s24, 6\n"
      "s_add_u32 s51, s51, s52\n"
      "s_lshl_b32 s53, s24, 11\n"
      "s_lshr_b32 s54, s25, 2\n"
      "s_lshl_b32 s54, s54, 9\n"
      "s_add_u32 s53, s53, s54\n"
      "s_and_b32 s54, s25, 3\n"
      "s_lshl_b32 s54, s54, 6\n"
      "buffer_load_dwordx4 v22, s[84:87], s67 offen offset:1024 lds\n"
      "s_add_u32 s53, s53, s54\n"
      "s_lshl_b32 s54, s25, 10\n"
      "s_add_u32 s54, s54, s52\n"
      "s_add_u32 s54, s54, 32768\n"
      "s_cmp_eq_u32 s73, 0\n"
      "s_cselect_b32 s57, s50, s51\n"
      "s_cselect_b32 s58, s53, s54\n"
      "s_cselect_b32 s50, 11, 15\n"
      "s_cselect_b32 s51, 8, 4\n"
      "s_cselect_b64 s[78:79], 0, -1\n"
      "s_add_u32 s57, s57, 1048576\n"
      "s_add_u32 s58, s58, 6291456\n"
      "s_cmp_lt_u32 s73, 2\n"
      "s_cselect_b32 s57, s57, 0x7fff0000\n"
      "s_cselect_b32 s58, s58, 0x7fff0000\n"
      "v_lshlrev_b32 v3, s50, v1\n"
      "v_or_b32 v3, v3, v2\n"
      "v_lshl_or_b32 v2, v1, 8, v2\n"
      "buffer_load_dwordx4 v23, s[84:87], s67 offen offset:2048 lds\n"
      "v_cmp_gt_u32 vcc, s51, v250\n"
      "v_mov_b32 v4, 0x7fff0000\n"
      "s_nop 0\n"
      "v_cndmask_b32 v2, v4, v2, vcc\n"
      "s_getpc_b64 s[0:1]\n"
      ".Lk_pc1:\n"
      "s_sub_u32 s0, s0, .Lk_pc1-.Lk_start\n"
      "s_subb_u32 s1, s1, 0\n"
      "s_lshl_b32 s52, s31, 2\n"
      "s_add_u32 s52, s52, s73\n"
      "v_lshl_add_u32 v145, v250, 7, s52\n"
      "v_lshlrev_b32 v145, 7, v145\n"
      "v_min_u32 v145, .Lk_end-.Lk_start-4, v145\n"
      "buffer_load_dwordx4 v24, s[84:87], s67 offen offset:3072 lds\n"
      "s_lshl_b32 s50, s25, 7\n"
      "s_add_u32 s50, s50, 47104\n"
      "v_lshl_add_u32 v62, v251, 3, s50\n"
      "s_lshl_b32 s77, s26, 14\n"
      "s_add_u32 s77, s77, s65\n"
      "s_add_u32 s77, s77, 2097152\n"
      "v_mov_b32 v152, -1\n"
      "v_mov_b32 v153, -1\n"
      "v_mov_b32 v154, -1\n"
      "v_mov_b32 v155, -1\n"
      "s_lshl_b32 s50, s73, 8\n"
      "s_add_u32 m0, s50, 135168\n"
      "s_mov_b64 exec, 0xffff\n"
      "buffer_load_dwordx4 v25, s[84:87], s68 offen lds\n"
      "s_mov_b64 exec, -1\n"
      "s_cmp_eq_u32 s64, 0\n"
      "s_cbranch_scc1 .Lk_xg0\n"
      ".Lk_xr0:\n"
      "s_waitcnt vmcnt(16)\n"
      "s_barrier\n"
      "s_add_u32 m0, s66, 45056\n"
      "s_nop 0\n"
      "buffer_load_dwordx4 v14, s[40:43], s63 offen lds\n"
      "buffer_load_dwordx4 v15, s[40:43], s63 offen offset:1024 lds\n"
      "buffer_load_dwordx4 v16, s[40:43], s63 offen offset:2048 lds\n"
      "buffer_load_dwordx4 v17, s[40:43], s63 offen offset:3072 lds\n"
      "s_add_u32 m0, s66, 49152\n"
      "s_nop 0\n"
      "buffer_load_dwordx4 v18, s[40:43], s63 offen lds\n"
      "buffer_load_dwordx4 v19, s[40:43], s63 offen offset:1024 lds\n"
      "buffer_load_dwordx4 v20, s[40:43], s63 offen offset:2048 lds\n"
      "s_add_u32 m0, s65, 45056\n"
      "s_nop 0\n"
      "buffer_load_dwordx4 v10, s[36:39], s62 offen lds\n"
      "buffer_load_dwordx4 v11, s[36:39], s62 offen offset:1024 lds\n"
      "v_readlane_b32 s60, v147, 4\n"
      "v_readlane_b32 s61, v148, 4\n"
      "buffer_load_dwordx4 v12, s[36:39], s62 offen offset:2048 lds\n"
      "buffer_load_dwordx4 v13, s[36:39], s62 offen offset:3072 lds\n"
      "s_cmp_eq_u32 s64, 1\n"
      "s_cbranch_scc1 .Lk_xg1\n"
      ".Lk_xr1:\n"
      "s_waitcnt vmcnt(11)\n"
      "s_barrier\n"
      "s_add_u32 m0, s66, 90112\n"
      "s_nop 0\n"
      "buffer_load_dwordx4 v14, s[40:43], s61 offen lds\n"
      "ds_read_b64 v[40:41], v27 offset:640\n"
      "buffer_load_dwordx4 v15, s[40:43], s61 offen offset:1024 lds\n"
      "ds_read_b64 v[42:43], v27 offset:768\n"
      "buffer_load_dwordx4 v16, s[40:43], s61 offen offset:2048 lds\n"
      "ds_read_b64 v[44:45], v27 offset:896\n"
      "buffer_load_dwordx4 v17, s[40:43], s61 offen offset:3072 lds\n"
      "ds_read_b64 v[46:47], v26 offset:0\n"
      "s_add_u32 m0, s66, 94208\n"
      "s_nop 0\n"
      "buffer_load_dwordx4 v18, s[40:43], s61 offen lds\n"
      "ds_read_b64 v[48:49], v26 offset:128\n"
      "ds_read_b64 v[50:51], v26 offset:256\n"
      "buffer_load_dwordx4 v19, s[40:43], s61 offen offset:1024 lds\n"
      "ds_read_b64 v[52:53], v26 offset:384\n"
      "buffer_load_dwordx4 v20, s[40:43], s61 offen offset:2048 lds\n"
      "ds_read_b64 v[54:55], v26 offset:512\n"
      "s_add_u32 m0, s65, 90112\n"
      "s_nop 0\n"
      "buffer_load_dwordx4 v10, s[36:39], s60 offen lds\n"
      "ds_read_b64 v[56:57], v26 offset:640\n"
      "buffer_load_dwordx4 v11, s[36:39], s60 offen offset:1024 lds\n"
      "v_readlane_b32 s62, v147, 5\n"
      "v_readlane_b32 s63, v148, 5\n"
      "ds_read_b64 v[58:59], v26 offset:768\n"
      "buffer_load_dwordx4 v12, s[36:39], s60 offen offset:2048 lds\n"
      "ds_read_b64 v[60:61], v26 offset:896\n"
      "buffer_load_dwordx4 v13, s[36:39], s60 offen offset:3072 lds\n"
      "s_cmp_eq_u32 s64, 2\n"
      "s_cbranch_scc1 .Lk_xg2\n"
      ".Lk_xr2:\n"
      "s_waitcnt vmcnt(11)\n"
      "s_waitcnt lgkmcnt(0)\n"
      "s_barrier\n"
      "s_add_u32 m0, s66, 0\n"
      "s_nop 0\n"
      "buffer_load_dwordx4 v14, s[40:43], s63 offen lds\n"
      "v_lshlrev_b32 v96, 16, v32\n"
      "v_and_b32 v97, v236, v32\n"
      "v_lshlrev_b32 v98, 16, v33\n"
      "buffer_load_dwordx4 v15, s[40:43], s63 offen offset:1024 lds\n"
      "v_and_b32 v99, v236, v33\n"
      "v_lshlrev_b32 v100, 16, v40\n"
      "v_lshlrev_b32 v101, 16, v42\n"
      "v_lshlrev_b32 v102, 16, v44\n"
      "buffer_load_dwordx4 v16, s[40:43], s63 offen offset:2048 lds\n"
      "v_lshlrev_b32 v103, 16, v46\n"
      "v_lshlrev_b32 v104, 16, v48\n"
      "v_lshlrev_b32 v105, 16, v50\n"
      "buffer_load_dwordx4 v17, s[40:43], s63 offen offset:3072 lds\n"
      "v_lshlrev_b32 v106, 16, v52\n"
      "v_lshlrev_b32 v107, 16, v54\n"
      "v_lshlrev_b32 v108, 16, v56\n"
      "s_add_u32 m0, s66, 4096\n"
      "s_nop 0\n"
      "buffer_load_dwordx4 v18, s[40:43], s63 offen lds\n"
      "v_lshlrev_b32 v109, 16, v58\n"
      "v_lshlrev_b32 v110, 16, v60\n"
      "v_mul_f32 v64, v100, v99\n"
      "v_mul_f32 v65, v101, v99\n"
      "buffer_load_dwordx4 v19, s[40:43], s63 offen offset:1024 lds\n"
      "v_mul_f32 v66, v102, v99\n"
      "v_mul_f32 v67, v103, v99\n"
      "v_mul_f32 v68, v104, v99\n"
      "buffer_load_dwordx4 v20, s[40:43], s63 offen offset:2048 lds\n"
      "v_mul_f32 v69, v105, v99\n"
      "v_mul_f32 v70, v106, v99\n"
      "v_mul_f32 v71, v107, v99\n"
      "v_fmac_f32 v64, v101, v98\n"
      "s_add_u32 m0, s65, 0\n"
      "s_nop 0\n"
      "buffer_load_dwordx4 v10, s[36:39], s62 offen lds\n"
      "v_fmac_f32 v65, v102, v98\n"
      "v_fmac_f32 v66, v103, v98\n"
      "v_fmac_f32 v67, v104, v98\n"
      "buffer_load_dwordx4 v11, s[36:39], s62 offen offset:1024 lds\n"
      "v_readlane_b32 s60, v147, 6\n"
      "v_readlane_b32 s61, v148, 6\n"
      "v_fmac_f32 v68, v105, v98\n"
      "v_fmac_f32 v69, v106, v98\n"
      "v_fmac_f32 v70, v107, v98\n"
      "buffer_load_dwordx4 v12, s[36:39], s62 offen offset:2048 lds\n"
      "v_fmac_f32 v71, v108, v98\n"
      "v_fmac_f32 v64, v102, v97\n"
      "v_fmac_f32 v65, v103, v97\n"
      "v_fmac_f32 v66, v104, v97\n"
      "buffer_load_dwordx4 v13, s[36:39], s62 offen offset:3072 lds\n"
      "v_fmac_f32 v67, v105, v97\n"
      "v_fmac_f32 v68, v106, v97\n"
      "v_fmac_f32 v69, v107, v97\n"
      "s_mov_b32 m0, s69\n"
      "s_nop 0\n"
      "buffer_load_dwordx4 v21, s[44:47], s67 offen lds\n"
      "v_fmac_f32 v70, v108, v97\n"
      "v_fmac_f32 v71, v109, v97\n"
      "v_fmac_f32 v64, v103, v96\n"
      "buffer_load_dwordx4 v22, s[44:47], s67 offen offset:1024 lds\n"
      "v_fmac_f32 v65, v104, v96\n"
      "v_fmac_f32 v66, v105, v96\n"
      "v_fmac_f32 v67, v106, v96\n"
      "v_fmac_f32 v68, v107, v96\n"
      "buffer_load_dwordx4 v23, s[44:47], s67 offen offset:2048 lds\n"
      "v_fmac_f32 v69, v108, v96\n"
      "v_fmac_f32 v70, v109, v96\n"
      "v_fmac_f32 v71, v110, v96\n"
      "buffer_load_dwordx4 v24, s[44:47], s67 offen offset:3072 lds\n"
      "s_cmp_eq_u32 s64, 3\n"
      "s_cbranch_scc1 .Lk_xg3\n"
      ".Lk_xr3:\n"
      "s_waitcnt vmcnt(15)\n"
      "s_barrier\n"
      "s_add_u32 s50, s71, 4096\n"
      "s_mov_b64 exec, 3\n"
      "buffer_store_dwordx2 v[248:249], v144, s[32:35], s50 offen sc1\n"
      "s_mov_b64 exec, -1\n"
      "v_lshlrev_b32 v96, 16, v34\n"
      "v_and_b32 v97, v236, v34\n"
      "v_lshlrev_b32 v98, 16, v35\n"
      "v_and_b32 v99, v236, v35\n"
      "s_add_u32 m0, s66, 45056\n"
      "s_nop 0\n"
      "buffer_load_dwordx4 v14, s[40:43], s61 offen lds\n"
      "v_and_b32 v100, v236, v40\n"
      "v_and_b32 v101, v236, v42\n"
      "v_and_b32 v102, v236, v44\n"
      "v_and_b32 v103, v236, v46\n"
      "v_and_b32 v104, v236, v48\n"
      "buffer_load_dwordx4 v15, s[40:43], s61 offen offset:1024 lds\n"
      "v_and_b32 v105, v236, v50\n"
      "v_and_b32 v106, v236, v52\n"
      "v_and_b32 v107, v236, v54\n"
      "v_and_b32 v108, v236, v56\n"
      "buffer_load_dwordx4 v16, s[40:43], s61 offen offset:2048 lds\n"
      "v_and_b32 v109, v236, v58\n"
      "v_and_b32 v110, v236, v60\n"
      "v_mul_f32 v72, v100, v99\n"
      "v_mul_f32 v73, v101, v99\n"
      "buffer_load_dwordx4 v17, s[40:43], s61 offen offset:3072 lds\n"
      "v_mul_f32 v74, v102, v99\n"
      "v_mul_f32 v75, v103, v99\n"
      "v_mul_f32 v76, v104, v99\n"
      "v_mul_f32 v77, v105, v99\n"
      "s_add_u32 m0, s66, 49152\n"
      "s_nop 0\n"
      "buffer_load_dwordx4 v18, s[40:43], s61 offen lds\n"
      "v_mul_f32 v78, v106, v99\n"
      "v_mul_f32 v79, v107, v99\n"
      "v_fmac_f32 v72, v101, v98\n"
      "v_fmac_f32 v73, v102, v98\n"
      "v_fmac_f32 v74, v103, v98\n"
      "buffer_load_dwordx4 v19, s[40:43], s61 offen offset:1024 lds\n"
      "v_fmac_f32 v75, v104, v98\n"
      "v_fmac_f32 v76, v105, v98\n"
      "v_fmac_f32 v77, v106, v98\n"
      "v_fmac_f32 v78, v107, v98\n"
      "buffer_load_dwordx4 v20, s[40:43], s61 offen offset:2048 lds\n"
      "v_fmac_f32 v79, v108, v98\n"
      "v_fmac_f32 v72, v102, v97\n"
      "v_fmac_f32 v73, v103, v97\n"
      "v_fmac_f32 v74, v104, v97\n"
      "s_add_u32 m0, s65, 45056\n"
      "s_nop 0\n"
      "buffer_load_dwordx4 v10, s[36:39], s60 offen lds\n"
      "v_fmac_f32 v75, v105, v97\n"
      "v_fmac_f32 v76, v106, v97\n"
      "v_fmac_f32 v77, v107, v97\n"
      "v_fmac_f32 v78, v108, v97\n"
      "buffer_load_dwordx4 v11, s[36:39], s60 offen offset:1024 lds\n"
      "v_readlane_b32 s62, v147, 7\n"
      "v_readlane_b32 s63, v148, 7\n"
      "v_fmac_f32 v79, v109, v97\n"
      "v_fmac_f32 v72, v103, v96\n"
      "v_fmac_f32 v73, v104, v96\n"
      "v_fmac_f32 v74, v105, v96\n"
      "v_fmac_f32 v75, v106, v96\n"
      "buffer_load_dwordx4 v12, s[36:39], s60 offen offset:2048 lds\n"
      "v_fmac_f32 v76, v107, v96\n"
      "v_fmac_f32 v77, v108, v96\n"
      "v_fmac_f32 v78, v109, v96\n"
      "v_fmac_f32 v79, v110, v96\n"
      "buffer_load_dwordx4 v13, s[36:39], s60 offen offset:3072 lds\n"
      "s_cmp_eq_u32 s64, 4\n"
      "s_cbranch_scc1 .Lk_xg4\n"
      ".Lk_xr4:\n"
      "s_waitcnt vmcnt(12)\n"
      "s_barrier\n"
      "s_add_u32 m0, s66, 90112\n"
      "s_nop 0\n"
      "buffer_load_dwordx4 v14, s[40:43], s63 offen lds\n"
      "ds_read_b64 v[112:113], v26 offset:0\n"
      "ds_read_b64 v[114:115], v26 offset:128\n"
      "ds_read_b64 v[116:117], v26 offset:256\n"
      "ds_read_b64 v[118:119], v26 offset:384\n"
      "ds_read_b64 v[120:121], v26 offset:512\n"
      "ds_read_b64 v[122:123], v26 offset:640\n"
      "buffer_load_dwordx4 v15, s[40:43], s63 offen offset:1024 lds\n"
      "ds_read_b64 v[124:125], v26 offset:768\n"
      "ds_read_b64 v[126:127], v26 offset:896\n"
      "v_lshlrev_b32 v96, 16, v36\n"
      "v_and_b32 v97, v236, v36\n"
      "v_lshlrev_b32 v98, 16, v37\n"
      "buffer_load_dwordx4 v16, s[40:43], s63 offen offset:2048 lds\n"
      "v_and_b32 v99, v236, v37\n"
      "v_lshlrev_b32 v100, 16, v41\n"
      "v_lshlrev_b32 v101, 16, v43\n"
      "v_lshlrev_b32 v102, 16, v45\n"
      "v_lshlrev_b32 v103, 16, v47\n"
      "buffer_load_dwordx4 v17, s[40:43], s63 offen offset:3072 lds\n"
      "v_lshlrev_b32 v104, 16, v49\n"
      "v_lshlrev_b32 v105, 16, v51\n"
      "v_lshlrev_b32 v106, 16, v53\n"
      "v_lshlrev_b32 v107, 16, v55\n"
      "v_lshlrev_b32 v108, 16, v57\n"
      "v_lshlrev_b32 v109, 16, v59\n"
      "s_add_u32 m0, s66, 94208\n"
      "s_nop 0\n"
      "buffer_load_dwordx4 v18, s[40:43], s63 offen lds\n"
      "v_lshlrev_b32 v110, 16, v61\n"
      "v_mul_f32 v80, v100, v99\n"
      "v_mul_f32 v81, v101, v99\n"
      "v_mul_f32 v82, v102, v99\n"
      "v_mul_f32 v83, v103, v99\n"
      "v_mul_f32 v84, v104, v99\n"
      "buffer_load_dwordx4 v19, s[40:43], s63 offen offset:1024 lds\n"
      "v_mul_f32 v85, v105, v99\n"
      "v_mul_f32 v86, v106, v99\n"
      "v_mul_f32 v87, v107, v99\n"
      "v_fmac_f32 v80, v101, v98\n"
      "v_fmac_f32 v81, v102, v98\n"
      "buffer_load_dwordx4 v20, s[40:43], s63 offen offset:2048 lds\n"
      "v_fmac_f32 v82, v103, v98\n"
      "v_fmac_f32 v83, v104, v98\n"
      "v_fmac_f32 v84, v105, v98\n"
      "v_fmac_f32 v85, v106, v98\n"
      "v_fmac_f32 v86, v107, v98\n"
      "s_add_u32 m0, s65, 90112\n"
      "s_nop 0\n"
      "buffer_load_dwordx4 v10, s[36:39], s62 offen lds\n"
      "v_fmac_f32 v87, v108, v98\n"
      "v_fmac_f32 v80, v102, v97\n"
      "v_fmac_f32 v81, v103, v97\n"
      "v_fmac_f32 v82, v104, v97\n"
      "v_fmac_f32 v83, v105, v97\n"
      "v_fmac_f32 v84, v106, v97\n"
      "buffer_load_dwordx4 v11, s[36:39], s62 offen offset:1024 lds\n"
      "v_readlane_b32 s60, v147, 8\n"
      "v_readlane_b32 s61, v148, 8\n"
      "v_fmac_f32 v85, v107, v97\n"
      "v_fmac_f32 v86, v108, v97\n"
      "v_fmac_f32 v87, v109, v97\n"
      "v_fmac_f32 v80, v103, v96\n"
      "v_fmac_f32 v81, v104, v96\n"
      "v_fmac_f32 v82, v105, v96\n"
      "buffer_load_dwordx4 v12, s[36:39], s62 offen offset:2048 lds\n"
      "v_fmac_f32 v83, v106, v96\n"
      "v_fmac_f32 v84, v107, v96\n"
      "v_fmac_f32 v85, v108, v96\n"
      "v_fmac_f32 v86, v109, v96\n"
      "v_fmac_f32 v87, v110, v96\n"
      "buffer_load_dwordx4 v13, s[36:39], s62 offen offset:3072 lds\n"
      "s_cmp_eq_u32 s64, 5\n"
      "s_cbranch_scc1 .Lk_xg5\n"
      ".Lk_xr5:\n"
      "s_waitcnt vmcnt(11)\n"
      "s_waitcnt lgkmcnt(0)\n"
      "s_barrier\n"
      "s_add_u32 m0, s66, 0\n"
      "s_nop 0\n"
      "buffer_load_dwordx4 v14, s[40:43], s61 offen lds\n"
      "v_lshlrev_b32 v96, 16, v38\n"
      "v_and_b32 v97, v236, v38\n"
      "v_lshlrev_b32 v98, 16, v39\n"
      "v_and_b32 v99, v236, v39\n"
      "v_and_b32 v100, v236, v41\n"
      "v_and_b32 v101, v236, v43\n"
      "v_and_b32 v102, v236, v45\n"
      "buffer_load_dwordx4 v15, s[40:43], s61 offen offset:1024 lds\n"
      "v_and_b32 v103, v236, v47\n"
      "v_and_b32 v104, v236, v49\n"
      "v_and_b32 v105, v236, v51\n"
      "v_and_b32 v106, v236, v53\n"
      "v_and_b32 v107, v236, v55\n"
      "v_and_b32 v108, v236, v57\n"
      "v_and_b32 v109, v236, v59\n"
      "buffer_load_dwordx4 v16, s[40:43], s61 offen offset:2048 lds\n"
      "v_and_b32 v110, v236, v61\n"
      "v_mul_f32 v88, v100, v99\n"
      "v_mul_f32 v89, v101, v99\n"
      "v_mul_f32 v90, v102, v99\n"
      "v_mul_f32 v91, v103, v99\n"
      "v_mul_f32 v92, v104, v99\n"
      "buffer_load_dwordx4 v17, s[40:43], s61 offen offset:3072 lds\n"
      "v_mul_f32 v93, v105, v99\n"
      "v_mul_f32 v94, v106, v99\n"
      "v_mul_f32 v95, v107, v99\n"
      "v_fmac_f32 v88, v101, v98\n"
      "v_fmac_f32 v89, v102, v98\n"
      "v_fmac_f32 v90, v103, v98\n"
      "v_fmac_f32 v91, v104, v98\n"
      "s_add_u32 m0, s66, 4096\n"
      "s_nop 0\n"
      "buffer_load_dwordx4 v18, s[40:43], s61 offen lds\n"
      "v_fmac_f32 v92, v105, v98\n"
      "v_fmac_f32 v93, v106, v98\n"
      "v_fmac_f32 v94, v107, v98\n"
      "v_fmac_f32 v95, v108, v98\n"
      "v_fmac_f32 v88, v102, v97\n"
      "v_fmac_f32 v89, v103, v97\n"
      "v_fmac_f32 v90, v104, v97\n"
      "buffer_load_dwordx4 v19, s[40:43], s61 offen offset:1024 lds\n"
      "v_fmac_f32 v91, v105, v97\n"
      "v_fmac_f32 v92, v106, v97\n"
      "v_fmac_f32 v93, v107, v97\n"
      "v_fmac_f32 v94, v108, v97\n"
      "v_fmac_f32 v95, v109, v97\n"
      "v_fmac_f32 v88, v103, v96\n"
      "v_fmac_f32 v89, v104, v96\n"
      "buffer_load_dwordx4 v20, s[40:43], s61 offen offset:2048 lds\n"
      "v_fmac_f32 v90, v105, v96\n"
      "v_fmac_f32 v91, v106, v96\n"
      "v_fmac_f32 v92, v107, v96\n"
      "v_fmac_f32 v93, v108, v96\n"
      "v_fmac_f32 v94, v109, v96\n"
      "v_fmac_f32 v95, v110, v96\n"
      "v_lshlrev_b32 v96, 16, v112\n"
      "s_add_u32 m0, s65, 0\n"
      "s_nop 0\n"
      "buffer_load_dwordx4 v10, s[36:39], s60 offen lds\n"
      "v_lshlrev_b32 v97, 16, v114\n"
      "v_lshlrev_b32 v98, 16, v116\n"
      "v_lshlrev_b32 v99, 16, v118\n"
      "v_lshlrev_b32 v100, 16, v120\n"
      "v_lshlrev_b32 v101, 16, v122\n"
      "v_lshlrev_b32 v102, 16, v124\n"
      "buffer_load_dwordx4 v11, s[36:39], s60 offen offset:1024 lds\n"
      "v_readlane_b32 s62, v147, 9\n"
      "v_readlane_b32 s63, v148, 9\n"
      "v_lshlrev_b32 v103, 16, v126\n"
      "v_mul_f32 v96, v64, v96\n"
      "v_mul_f32 v97, v65, v97\n"
      "v_mul_f32 v98, v66, v98\n"
      "v_mul_f32 v99, v67, v99\n"
      "v_mul_f32 v100, v68, v100\n"
      "v_mul_f32 v101, v69, v101\n"
      "buffer_load_dwordx4 v12, s[36:39], s60 offen offset:2048 lds\n"
      "v_mul_f32 v102, v70, v102\n"
      "v_mul_f32 v103, v71, v103\n"
      "v_cvt_pk_bf16_f32 v64, v103, v102\n"
      "v_cvt_pk_bf16_f32 v65, v101, v100\n"
      "v_cvt_pk_bf16_f32 v66, v99, v98\n"
      "v_cvt_pk_bf16_f32 v67, v97, v96\n"
      "buffer_store_dwordx4 v[64:67], v28, s[32:35], s70 offen sc1\n"
      "s_add_u32 s70, s70, 4096\n"
      "buffer_load_dwordx4 v13, s[36:39], s60 offen offset:3072 lds\n"
      "s_cmp_eq_u32 s64, 6\n"
      "s_cbranch_scc1 .Lk_xg6\n"
      ".Lk_xr6:\n"
      "s_waitcnt vmcnt(12)\n"
      "s_barrier\n"
      "buffer_load_dwordx2 v[30:31], v29, s[32:35], 0 offen sc1\n"
      "v_and_b32 v96, v236, v112\n"
      "v_and_b32 v97, v236, v114\n"
      "s_add_u32 m0, s66, 45056\n"
      "s_nop 0\n"
      "buffer_load_dwordx4 v14, s[40:43], s63 offen lds\n"
      "v_and_b32 v98, v236, v116\n"
      "v_and_b32 v99, v236, v118\n"
      "buffer_load_dwordx4 v15, s[40:43], s63 offen offset:1024 lds\n"
      "v_and_b32 v100, v236, v120\n"
      "v_and_b32 v101, v236, v122\n"
      "buffer_load_dwordx4 v16, s[40:43], s63 offen offset:2048 lds\n"
      "v_and_b32 v102, v236, v124\n"
      "v_and_b32 v103, v236, v126\n"
      "buffer_load_dwordx4 v17, s[40:43], s63 offen offset:3072 lds\n"
      "v_mul_f32 v96, v72, v96\n"
      "v_mul_f32 v97, v73, v97\n"
      "s_add_u32 m0, s66, 49152\n"
      "s_nop 0\n"
      "buffer_load_dwordx4 v18, s[40:43], s63 offen lds\n"
      "v_mul_f32 v98, v74, v98\n"
      "buffer_load_dwordx4 v19, s[40:43], s63 offen offset:1024 lds\n"
      "v_mul_f32 v99, v75, v99\n"
      "v_mul_f32 v100, v76, v100\n"
      "buffer_load_dwordx4 v20, s[40:43], s63 offen offset:2048 lds\n"
      "v_mul_f32 v101, v77, v101\n"
      "v_mul_f32 v102, v78, v102\n"
      "s_add_u32 m0, s65, 45056\n"
      "s_nop 0\n"
      "buffer_load_dwordx4 v10, s[36:39], s62 offen lds\n"
      "v_mul_f32 v103, v79, v103\n"
      "v_cvt_pk_bf16_f32 v72, v103, v102\n"
      "buffer_load_dwordx4 v11, s[36:39], s62 offen offset:1024 lds\n"
      "v_readlane_b32 s60, v147, 10\n"
      "v_readlane_b32 s61, v148, 10\n"
      "v_cvt_pk_bf16_f32 v73, v101, v100\n"
      "v_cvt_pk_bf16_f32 v74, v99, v98\n"
      "buffer_load_dwordx4 v12, s[36:39], s62 offen offset:2048 lds\n"
      "v_cvt_pk_bf16_f32 v75, v97, v96\n"
      "buffer_store_dwordx4 v[72:75], v28, s[32:35], s70 offen sc1\n"
      "s_add_u32 s70, s70, 4096\n"
      "buffer_load_dwordx4 v13, s[36:39], s62 offen offset:3072 lds\n"
      "s_cmp_eq_u32 s64, 7\n"
      "s_cbranch_scc1 .Lk_xg7\n"
      ".Lk_xr7:\n"
      "s_waitcnt vmcnt(13)\n"
      "s_waitcnt vmcnt(12)\n"
      "v_cmp_ne_u64 vcc, s[48:49], v[30:31]\n"
      "s_cmp_lg_u32 s73, 0\n"
      "s_cbranch_scc1 .Lk_pc2\n"
      "s_cbranch_vccz .Lk_pc2\n"
      "s_mov_b32 s56, 60000\n"
      ".Lk_fw3:\n"
      "buffer_load_dwordx2 v[30:31], v29, s[32:35], 0 offen sc1\n"
      "s_waitcnt vmcnt(0)\n"
      "v_cmp_ne_u64 vcc, s[48:49], v[30:31]\n"
      "s_nop 0\n"
      "s_cbranch_vccz .Lk_fd4\n"
      "s_sleep 2\n"
      "s_sub_u32 s56, s56, 1\n"
      "s_cbranch_scc0 .Lk_fw3\n"
      ".Lk_fd4:\n"
      ".Lk_pc2:\n"
      "s_barrier\n"
      "buffer_load_dwordx4 v[128:131], v3, s[32:35], s57 offen sc1\n"
      "v_lshlrev_b32 v96, 16, v113\n"
      "v_lshlrev_b32 v97, 16, v115\n"
      "s_add_u32 m0, s66, 90112\n"
      "s_nop 0\n"
      "buffer_load_dwordx4 v14, s[40:43], s61 offen lds\n"
      "v_lshlrev_b32 v98, 16, v117\n"
      "v_lshlrev_b32 v99, 16, v119\n"
      "buffer_load_dwordx4 v15, s[40:43], s61 offen offset:1024 lds\n"
      "v_lshlrev_b32 v100, 16, v121\n"
      "v_lshlrev_b32 v101, 16, v123\n"
      "buffer_load_dwordx4 v16, s[40:43], s61 offen offset:2048 lds\n"
      "v_lshlrev_b32 v102, 16, v125\n"
      "v_lshlrev_b32 v103, 16, v127\n"
      "buffer_load_dwordx4 v17, s[40:43], s61 offen offset:3072 lds\n"
      "v_mul_f32 v96, v80, v96\n"
      "v_mul_f32 v97, v81, v97\n"
      "s_add_u32 m0, s66, 94208\n"
      "s_nop 0\n"
      "buffer_load_dwordx4 v18, s[40:43], s61 offen lds\n"
      "v_mul_f32 v98, v82, v98\n"
      "v_mul_f32 v99, v83, v99\n"
      "buffer_load_dwordx4 v19, s[40:43], s61 offen offset:1024 lds\n"
      "v_mul_f32 v100, v84, v100\n"
      "v_mul_f32 v101, v85, v101\n"
      "buffer_load_dwordx4 v20, s[40:43], s61 offen offset:2048 lds\n"
      "v_mul_f32 v102, v86, v102\n"
      "v_mul_f32 v103, v87, v103\n"
      "s_add_u32 m0, s65, 90112\n"
      "s_nop 0\n"
      "buffer_load_dwordx4 v10, s[36:39], s60 offen lds\n"
      "v_cvt_pk_bf16_f32 v80, v103, v102\n"
      "v_cvt_pk_bf16_f32 v81, v101, v100\n"
      "buffer_load_dwordx4 v11, s[36:39], s60 offen offset:1024 lds\n"
      "v_readlane_b32 s62, v147, 11\n"
      "v_readlane_b32 s63, v148, 11\n"
      "v_cvt_pk_bf16_f32 v82, v99, v98\n"
      "v_cvt_pk_bf16_f32 v83, v97, v96\n"
      "buffer_load_dwordx4 v12, s[36:39], s60 offen offset:2048 lds\n"
      "buffer_store_dwordx4 v[80:83], v28, s[32:35], s70 offen sc1\n"
      "s_add_u32 s70, s70, 4096\n"
      "v_add_u32 v29, 20480, v29\n"
      "buffer_load_dwordx4 v13, s[36:39], s60 offen offset:3072 lds\n"
      "s_cmp_eq_u32 s64, 8\n"
      "s_cbranch_scc1 .Lk_xg8\n"
      ".Lk_xr8:\n"
      "s_waitcnt vmcnt(12)\n"
      "s_barrier\n"
      "s_add_u32 m0, s66, 0\n"
      "s_nop 0\n"
      "buffer_load_dwordx4 v14, s[40:43], s63 offen lds\n"
      "v_and_b32 v96, v236, v113\n"
      "v_and_b32 v97, v236, v115\n"
      "v_and_b32 v98, v236, v117\n"
      "v_and_b32 v99, v236, v119\n"
      "v_and_b32 v100, v236, v121\n"
      "v_and_b32 v101, v236, v123\n"
      "v_and_b32 v102, v236, v125\n"
      "v_and_b32 v103, v236, v127\n"
      "buffer_load_dwordx4 v15, s[40:43], s63 offen offset:1024 lds\n"
      "v_mul_f32 v96, v88, v96\n"
      "v_mul_f32 v97, v89, v97\n"
      "v_mul_f32 v98, v90, v98\n"
      "v_mul_f32 v99, v91, v99\n"
      "v_mul_f32 v100, v92, v100\n"
      "v_mul_f32 v101, v93, v101\n"
      "v_mul_f32 v102, v94, v102\n"
      "v_mul_f32 v103, v95, v103\n"
      "buffer_load_dwordx4 v16, s[40:43], s63 offen offset:2048 lds\n"
      "v_cvt_pk_bf16_f32 v88, v103, v102\n"
      "v_cvt_pk_bf16_f32 v89, v101, v100\n"
      "v_cvt_pk_bf16_f32 v90, v99, v98\n"
      "v_cvt_pk_bf16_f32 v91, v97, v96\n"
      "buffer_store_dwordx4 v[88:91], v28, s[32:35], s70 offen sc1\n"
      "s_add_u32 s70, s70, 4096\n"
      "v_add_f32_dpp v128, v128, v128 row_ror:4 row_mask:0xf bank_mask:0xf\n"
      "v_add_f32_dpp v129, v129, v129 row_ror:4 row_mask:0xf bank_mask:0xf\n"
      "v_add_f32_dpp v130, v130, v130 row_ror:4 row_mask:0xf bank_mask:0xf\n"
      "buffer_load_dwordx4 v17, s[40:43], s63 offen offset:3072 lds\n"
      "v_add_f32_dpp v131, v131, v131 row_ror:4 row_mask:0xf bank_mask:0xf\n"
      "v_add_f32_dpp v128, v128, v128 row_ror:8 row_mask:0xf bank_mask:0xf\n"
      "v_add_f32_dpp v129, v129, v129 row_ror:8 row_mask:0xf bank_mask:0xf\n"
      "v_add_f32_dpp v130, v130, v130 row_ror:8 row_mask:0xf bank_mask:0xf\n"
      "v_add_f32_dpp v131, v131, v131 row_ror:8 row_mask:0xf bank_mask:0xf\n"
      "v_mov_b32 v132, v128\n"
      "v_mov_b32 v133, v129\n"
      "v_mov_b32 v134, v130\n"
      "s_add_u32 m0, s66, 4096\n"
      "s_nop 0\n"
      "buffer_load_dwordx4 v18, s[40:43], s63 offen lds\n"
      "v_mov_b32 v135, v131\n"
      "v_permlane16_swap_b32 v128, v132\n"
      "v_permlane16_swap_b32 v129, v133\n"
      "v_permlane16_swap_b32 v130, v134\n"
      "v_permlane16_swap_b32 v131, v135\n"
      "v_add_f32 v128, v128, v132\n"
      "v_add_f32 v129, v129, v133\n"
      "v_add_f32 v130, v130, v134\n"
      "buffer_load_dwordx4 v19, s[40:43], s63 offen offset:1024 lds\n"
      "v_add_f32 v131, v131, v135\n"
      "v_mov_b32 v132, v128\n"
      "v_mov_b32 v133, v129\n"
      "v_mov_b32 v134, v130\n"
      "v_mov_b32 v135, v131\n"
      "v_permlane32_swap_b32 v128, v132\n"
      "v_permlane32_swap_b32 v129, v133\n"
      "v_permlane32_swap_b32 v130, v134\n"
      "v_permlane32_swap_b32 v131, v135\n"
      "buffer_load_dwordx4 v20, s[40:43], s63 offen offset:2048 lds\n"
      "v_add_f32 v128, v128, v132\n"
      "v_add_f32 v129, v129, v133\n"
      "v_add_f32 v130, v130, v134\n"
      "v_add_f32 v131, v131, v135\n"
      "v_cmp_eq_u32 vcc, 1, v1\n"
      "s_nop 1\n"
      "v_cndmask_b32 v136, v128, v130, vcc\n"
      "v_cndmask_b32 v137, v129, v131, vcc\n"
      "v_mul_f32 v136, 0x3a800000, v136\n"
      "s_add_u32 m0, s65, 0\n"
      "s_nop 0\n"
      "buffer_load_dwordx4 v10, s[36:39], s62 offen lds\n"
      "v_mul_f32 v137, 0x3a800000, v137\n"
      "v_fma_f32 v137, -v136, v136, v137\n"
      "v_max_f32 v137, 0, v137\n"
      "v_cvt_pk_bf16_f32 v136, v136, v137\n"
      "v_and_b32 v137, v236, v136\n"
      "v_lshlrev_b32 v136, 16, v136\n"
      "v_add_f32 v137, 0x3727c5ac, v137\n"
      "v_cvt_pk_bf16_f32 v137, v137, v137\n"
      "buffer_load_dwordx4 v11, s[36:39], s62 offen offset:1024 lds\n"
      "v_readlane_b32 s60, v147, 12\n"
      "v_readlane_b32 s61, v148, 12\n"
      "v_and_b32 v137, v236, v137\n"
      "v_sqrt_f32 v137, v137\n"
      "v_sub_f32 v140, 0, v136\n"
      "v_cvt_pk_bf16_f32 v137, v137, v137\n"
      "v_and_b32 v142, v236, v137\n"
      "v_rcp_f32 v143, v142\n"
      "s_nop 0\n"
      "v_mul_f32 v141, 0xbfb8aa3b, v143\n"
      "buffer_load_dwordx4 v12, s[36:39], s62 offen offset:2048 lds\n"
      "v_cndmask_b32 v140, v140, v128, s[78:79]\n"
      "v_cndmask_b32 v141, v141, v129, s[78:79]\n"
      "v_cndmask_b32 v142, v142, v130, s[78:79]\n"
      "v_cndmask_b32 v143, v143, v131, s[78:79]\n"
      "buffer_store_dwordx4 v[140:143], v2, s[32:35], s58 offen sc1\n"
      "s_mov_b32 s84, s12\n"
      "s_and_b32 s85, s13, 0xffff\n"
      "s_lshl_b32 s72, s74, 12\n"
      "buffer_load_dwordx4 v13, s[36:39], s62 offen offset:3072 lds\n"
      "s_cmp_eq_u32 s64, 9\n"
      "s_cbranch_scc1 .Lk_xg9\n"
      ".Lk_xr9:\n"
      "s_waitcnt vmcnt(13)\n"
      "s_barrier\n"
      "s_add_u32 m0, s66, 45056\n"
      "s_nop 0\n"
      "buffer_load_dwordx4 v14, s[40:43], s61 offen lds\n"
      "buffer_load_dwordx4 v15, s[40:43], s61 offen offset:1024 lds\n"
      "buffer_load_dwordx4 v16, s[40:43], s61 offen offset:2048 lds\n"
      "buffer_load_dwordx4 v17, s[40:43], s61 offen offset:3072 lds\n"
      "s_add_u32 m0, s66, 49152\n"
      "s_nop 0\n"
      "buffer_load_dwordx4 v18, s[40:43], s61 offen lds\n"
      "buffer_load_dwordx4 v19, s[40:43], s61 offen offset:1024 lds\n"
      "buffer_load_dwordx4 v20, s[40:43], s61 offen offset:2048 lds\n"
      "s_add_u32 m0, s65, 45056\n"
      "s_nop 0\n"
      "buffer_load_dwordx4 v10, s[36:39], s60 offen lds\n"
      "buffer_load_dwordx4 v11, s[36:39], s60 offen offset:1024 lds\n"
      "v_readlane_b32 s62, v147, 13\n"
      "v_readlane_b32 s63, v148, 13\n"
      "buffer_load_dwordx4 v12, s[36:39], s60 offen offset:2048 lds\n"
      "buffer_load_dwordx4 v13, s[36:39], s60 offen offset:3072 lds\n"
      "s_mov_b32 m0, s82\n"
      "s_nop 0\n"
      "buffer_load_dwordx4 v253, s[84:87], s72 offen lds\n"
      "buffer_load_dwordx4 v253, s[84:87], s72 offen offset:1024 lds\n"
      "buffer_load_dwordx4 v253, s[84:87], s72 offen offset:2048 lds\n"
      "buffer_load_dwordx4 v253, s[84:87], s72 offen offset:3072 lds\n"
      "s_cmp_eq_u32 s64, 10\n"
      "s_cbranch_scc1 .Lk_xg10\n"
      ".Lk_xr10:\n"
      "s_waitcnt vmcnt(15)\n"
      "s_barrier\n"
      "s_add_u32 s50, s71, 24576\n"
      "s_mov_b64 exec, 3\n"
      "buffer_store_dwordx2 v[248:249], v144, s[32:35], s50 offen sc1\n"
      "s_mov_b64 exec, -1\n"
      "s_add_u32 s50, s71, 45056\n"
      "s_mov_b64 exec, 3\n"
      "buffer_store_dwordx2 v[248:249], v144, s[32:35], s50 offen sc1\n"
      "s_mov_b64 exec, -1\n"
      "s_add_u32 m0, s66, 90112\n"
      "s_nop 0\n"
      "buffer_load_dwordx4 v14, s[40:43], s63 offen lds\n"
      "buffer_load_dwordx4 v15, s[40:43], s63 offen offset:1024 lds\n"
      "buffer_load_dwordx4 v16, s[40:43], s63 offen offset:2048 lds\n"
      "buffer_load_dwordx4 v17, s[40:43], s63 offen offset:3072 lds\n"
      "s_add_u32 m0, s66, 94208\n"
      "s_nop 0\n"
      "buffer_load_dwordx4 v18, s[40:43], s63 offen lds\n"
      "buffer_load_dwordx4 v19, s[40:43], s63 offen offset:1024 lds\n"
      "buffer_load_dwordx4 v20, s[40:43], s63 offen offset:2048 lds\n"
      "s_add_u32 m0, s65, 90112\n"
      "s_nop 0\n"
      "buffer_load_dwordx4 v10, s[36:39], s62 offen lds\n"
      "buffer_load_dwordx4 v11, s[36:39], s62 offen offset:1024 lds\n"
      "v_readlane_b32 s60, v147, 14\n"
      "v_readlane_b32 s61, v148, 14\n"
      "buffer_load_dwordx4 v12, s[36:39], s62 offen offset:2048 lds\n"
      "buffer_load_dwordx4 v13, s[36:39], s62 offen offset:3072 lds\n"
      "s_cmp_eq_u32 s64, 11\n"
      "s_cbranch_scc1 .Lk_xg11\n"
      ".Lk_xr11:\n"
      "s_waitcnt vmcnt(17)\n"
      "s_barrier\n"
      "s_add_u32 m0, s66, 0\n"
      "s_nop 0\n"
      "buffer_load_dwordx4 v14, s[40:43], s61 offen lds\n"
      "buffer_load_dwordx4 v15, s[40:43], s61 offen offset:1024 lds\n"
      "buffer_load_dwordx4 v16, s[40:43], s61 offen offset:2048 lds\n"
      "buffer_load_dwordx4 v17, s[40:43], s61 offen offset:3072 lds\n"
      "s_add_u32 m0, s66, 4096\n"
      "s_nop 0\n"
      "buffer_load_dwordx4 v18, s[40:43], s61 offen lds\n"
      "buffer_load_dwordx4 v19, s[40:43], s61 offen offset:1024 lds\n"
      "buffer_load_dwordx4 v20, s[40:43], s61 offen offset:2048 lds\n"
      "s_add_u32 m0, s65, 0\n"
      "s_nop 0\n"
      "buffer_load_dwordx4 v10, s[36:39], s60 offen lds\n"
      "buffer_load_dwordx4 v11, s[36:39], s60 offen offset:1024 lds\n"
      "v_readlane_b32 s62, v147, 15\n"
      "v_readlane_b32 s63, v148, 15\n"
      "buffer_load_dwordx4 v12, s[36:39], s60 offen offset:2048 lds\n"
      "buffer_load_dwordx4 v13, s[36:39], s60 offen offset:3072 lds\n"
      "buffer_store_dwordx4 v[152:155], v253, s[32:35], s77 offen offset:0 sc1\n"
      "s_cmp_eq_u32 s64, 12\n"
      "s_cbranch_scc1 .Lk_xg12\n"
      ".Lk_xr12:\n"
      "s_waitcnt vmcnt(12)\n"
      "s_barrier\n"
      "s_mov_b64 exec, 7\n"
      "global_load_dword v146, v145, s[0:1]\n"
      "s_mov_b64 exec, -1\n"
      "s_add_u32 m0, s66, 45056\n"
      "s_nop 0\n"
      "buffer_load_dwordx4 v14, s[40:43], s63 offen lds\n"
      "buffer_load_dwordx4 v15, s[40:43], s63 offen offset:1024 lds\n"
      "buffer_load_dwordx4 v16, s[40:43], s63 offen offset:2048 lds\n"
      "buffer_load_dwordx4 v17, s[40:43], s63 offen offset:3072 lds\n"
      "s_add_u32 m0, s66, 49152\n"
      "s_nop 0\n"
      "buffer_load_dwordx4 v18, s[40:43], s63 offen lds\n"
      "buffer_load_dwordx4 v19, s[40:43], s63 offen offset:1024 lds\n"
      "v_cmp_gt_u32 vcc, 32, v250\n"
      "s_nop 1\n"
      "v_cndmask_b32 v63, v62, v29, vcc\n"
      "buffer_load_dwordx4 v20, s[40:43], s63 offen offset:2048 lds\n"
      "s_add_u32 m0, s65, 45056\n"
      "s_nop 0\n"
      "buffer_load_dwordx4 v10, s[36:39], s62 offen lds\n"
      "buffer_load_dwordx4 v11, s[36:39], s62 offen offset:1024 lds\n"
      "buffer_load_dwordx4 v12, s[36:39], s62 offen offset:2048 lds\n"
      "buffer_load_dwordx4 v13, s[36:39], s62 offen offset:3072 lds\n"
      "buffer_store_dwordx4 v[152:155], v253, s[32:35], s77 offen offset:1024 sc1\n"
      "s_cmp_eq_u32 s64, 13\n"
      "s_cbranch_scc1 .Lk_xg13\n"
      ".Lk_xr13:\n"
      "s_waitcnt vmcnt(14)\n"
      "s_barrier\n"
      "buffer_load_dwordx2 v[30:31], v63, s[32:35], 0 offen sc1\n"
      "buffer_store_dwordx4 v[152:155], v253, s[32:35], s77 offen offset:2048 sc1\n"
      "buffer_store_dwordx4 v[152:155], v253, s[32:35], s77 offen offset:3072 sc1\n"
      "s_add_u32 m0, s75, 9236\n"
      "s_nop 0\n"
      "buffer_load_dwordx4 v253, s[84:87], s72 offen lds\n"
      "buffer_load_dwordx4 v253, s[84:87], s72 offen offset:1024 lds\n"
      "buffer_load_dwordx4 v253, s[84:87], s72 offen offset:2048 lds\n"
      "buffer_load_dwordx4 v253, s[84:87], s72 offen offset:3072 lds\n"
      "s_mov_b32 s40, s18\n"
      "s_and_b32 s41, s19, 0xffff\n"
      "s_mov_b32 s42, 7340032\n"
      "s_mov_b32 s43, 0x20000\n"
      "s_mov_b32 s36, s20\n"
      "s_and_b32 s37, s21, 0xffff\n"
      "s_mov_b32 s38, 4194304\n"
      "s_mov_b32 s39, 0x20000\n"
      "s_and_b32 s50, s24, 7\n"
      "s_add_u32 s50, s50, 0\n"
      "s_and_b32 s50, s50, 7\n"
      "s_lshr_b32 s51, s50, 1\n"
      "s_mul_i32 s51, s51, -5\n"
      "s_lshr_b32 s52, s50, 0\n"
      "s_mul_i32 s52, s52, 5\n"
      "s_add_u32 s50, s50, s51\n"
      "s_add_u32 s50, s50, s52\n"
      "s_lshl_b32 s60, s50, 17\n"
      "s_mov_b32 s66, s83\n"
      "s_lshl_b32 s50, s24, 11\n"
      "s_lshl_b32 s51, s73, 10\n"
      "s_add_u32 s50, s50, s51\n"
      "s_lshl_b32 s52, s25, 10\n"
      "s_add_u32 s52, s52, 32768\n"
      "s_cmp_lt_u32 s73, 2\n"
      "s_cselect_b32 s54, s50, s52\n"
      "s_cselect_b32 s51, s51, 2048\n"
      "s_add_u32 s54, s54, 6291456\n"
      "s_add_u32 m0, s51, 160768\n"
      "s_add_u32 s55, s72, 8388608\n"
      "s_cmp_eq_u32 s64, 14\n"
      "s_cbranch_scc1 .Lk_xg14\n"
      ".Lk_xr14:\n"
      "s_waitcnt vmcnt(6)\n"
      "v_cmp_ne_u64 s[80:81], s[48:49], v[30:31]\n"
      "s_cmp_lg_u32 s73, 0\n"
      "s_cbranch_scc1 .Lk_t1ok\n"
      "s_cmp_eq_u32 s80, 0\n"
      "s_cbranch_scc1 .Lk_t1ok\n"
      "s_mov_b32 s56, 60000\n"
      ".Lk_fw5:\n"
      "buffer_load_dwordx2 v[30:31], v29, s[32:35], 0 offen sc1\n"
      "s_waitcnt vmcnt(0)\n"
      "v_cmp_ne_u64 vcc, s[48:49], v[30:31]\n"
      "s_nop 0\n"
      "s_cbranch_vccz .Lk_fd6\n"
      "s_sleep 2\n"
      "s_sub_u32 s56, s56, 1\n"
      "s_cbranch_scc0 .Lk_fw5\n"
      ".Lk_fd6:\n"
      ".Lk_t1ok:\n"
      "s_barrier\n"
      "buffer_load_dwordx4 v253, s[32:35], s54 offen sc1 lds\n"
      "s_cmp_eq_u32 s81, 0\n"
      "s_cselect_b32 s53, s55, 0x7fff0000\n"
      "s_mov_b32 m0, s75\n"
      "s_nop 0\n"
      "buffer_load_dwordx4 v172, s[32:35], s53 offen sc1 lds\n"
      "buffer_load_dwordx4 v172, s[32:35], s53 offen offset:1024 sc1 lds\n"
      "buffer_load_dwordx4 v172, s[32:35], s53 offen offset:2048 sc1 lds\n"
      "buffer_load_dwordx4 v172, s[32:35], s53 offen offset:3072 sc1 lds\n"
      "s_add_u32 m0, s66, 0\n"
      "s_nop 0\n"
      "buffer_load_dwordx4 v200, s[40:43], s60 offen lds\n"
      "buffer_load_dwordx4 v201, s[40:43], s60 offen offset:1032 lds\n"
      "buffer_load_dwordx4 v202, s[40:43], s60 offen offset:2064 lds\n"
      "buffer_load_dwordx4 v203, s[40:43], s60 offen offset:3096 lds\n"
      "s_cmp_eq_u32 s64, 15\n"
      "s_cbranch_scc1 .Lk_xg15\n"
      ".Lk_xr15:\n"
      "s_waitcnt vmcnt(8)\n"
      "s_barrier\n"
      "s_cmp_eq_u32 s81, 0\n"
      "s_cbranch_scc1 .Lk_uok\n"
      "s_mov_b32 s56, 60000\n"
      ".Lk_fw7:\n"
      "buffer_load_dwordx2 v[30:31], v62, s[32:35], 0 offen sc1\n"
      "s_waitcnt vmcnt(0)\n"
      "v_cmp_ne_u64 vcc, s[48:49], v[30:31]\n"
      "s_nop 0\n"
      "s_cbranch_vccz .Lk_fd8\n"
      "s_sleep 4\n"
      "s_sub_u32 s56, s56, 1\n"
      "s_cbranch_scc0 .Lk_fw7\n"
      ".Lk_fd8:\n"
      "s_mov_b32 m0, s75\n"
      "s_nop 0\n"
      "buffer_load_dwordx4 v172, s[32:35], s55 offen sc1 lds\n"
      "buffer_load_dwordx4 v172, s[32:35], s55 offen offset:1024 sc1 lds\n"
      "buffer_load_dwordx4 v172, s[32:35], s55 offen offset:2048 sc1 lds\n"
      "buffer_load_dwordx4 v172, s[32:35], s55 offen offset:3072 sc1 lds\n"
      "s_waitcnt vmcnt(0)\n"
      ".Lk_uok:\n"
      "s_waitcnt vmcnt(4)\n"
      "s_sleep 3\n"
      "ds_write_b64 v175, v[218:219] offset:4096\n"
      "ds_write_b64 v175, v[218:219] offset:8720\n"
      "ds_write_b32 v166, v218\n"
      "ds_read_b128 v[32:35], v174 offset:0\n"
      "ds_read_b32 v48, v173 offset:0\n"
      "ds_read_b128 v[36:39], v174 offset:1024\n"
      "ds_read_b32 v49, v173 offset:1024\n"
      "ds_read_b128 v[40:43], v174 offset:2048\n"
      "ds_read_b32 v50, v173 offset:2048\n"
      "ds_read_b128 v[44:47], v174 offset:3072\n"
      "ds_read_b32 v51, v173 offset:3072\n"
      "s_waitcnt lgkmcnt(6)\n"
      "v_alignbit_b32 v32, v33, v32, 16\n"
      "v_alignbit_b32 v33, v34, v33, 16\n"
      "v_alignbit_b32 v34, v35, v34, 16\n"
      "v_alignbit_b32 v35, v48, v35, 16\n"
      "ds_write_b128 v174, v[32:35] offset:4624\n"
      "s_waitcnt lgkmcnt(5)\n"
      "v_alignbit_b32 v36, v37, v36, 16\n"
      "v_alignbit_b32 v37, v38, v37, 16\n"
      "v_alignbit_b32 v38, v39, v38, 16\n"
      "v_alignbit_b32 v39, v49, v39, 16\n"
      "ds_write_b128 v174, v[36:39] offset:5648\n"
      "s_waitcnt lgkmcnt(4)\n"
      "v_alignbit_b32 v40, v41, v40, 16\n"
      "v_alignbit_b32 v41, v42, v41, 16\n"
      "v_alignbit_b32 v42, v43, v42, 16\n"
      "v_alignbit_b32 v43, v50, v43, 16\n"
      "ds_write_b128 v174, v[40:43] offset:6672\n"
      "s_waitcnt lgkmcnt(3)\n"
      "v_alignbit_b32 v44, v45, v44, 16\n"
      "v_alignbit_b32 v45, v46, v45, 16\n"
      "v_alignbit_b32 v46, v47, v46, 16\n"
      "v_alignbit_b32 v47, v51, v47, 16\n"
      "ds_write_b128 v174, v[44:47] offset:7696\n"
      "s_waitcnt lgkmcnt(0)\n"
      "s_mov_b32 s60, 12\n"
      "s_mov_b32 s61, 1\n"
      "s_mov_b32 s63, 32896\n"
      "s_mov_b32 s64, 4294901504\n"
      "s_lshl_b32 s50, s26, 14\n"
      "s_lshl_b32 s51, s73, 12\n"
      "s_add_u32 s50, s50, s51\n"
      "s_add_u32 s50, s50, 2097152\n"
      "s_xor_b32 s51, s50, 131072\n"
      "s_add_u32 s68, s72, 8388608\n"
      "v_lshrrev_b32 v165, 3, v161\n"
      "v_and_b32 v165, 32, v165\n"
      "v_add3_u32 v163, v161, v165, v162\n"
      "v_xor_b32 v165, 32, v165\n"
      "v_add3_u32 v164, v161, v165, v162\n"
      "ds_read_b64 v[64:65], v163 offset:3584\n"
      "ds_read_b64 v[66:67], v164 offset:3584\n"
      "ds_read_b64 v[32:33], v160 offset:0\n"
      "ds_read_b64 v[34:35], v160 offset:32\n"
      "ds_read_b64 v[68:69], v163 offset:3072\n"
      "ds_read_b64 v[70:71], v164 offset:3072\n"
      "ds_read_b64 v[36:37], v160 offset:512\n"
      "ds_read_b64 v[38:39], v160 offset:544\n"
      "ds_read_b64 v[72:73], v163 offset:2560\n"
      "ds_read_b64 v[74:75], v164 offset:2560\n"
      "ds_read_b64 v[40:41], v160 offset:1024\n"
      "ds_read_b64 v[42:43], v160 offset:1056\n"
      "ds_read_b64 v[76:77], v163 offset:2048\n"
      "ds_read_b64 v[78:79], v164 offset:2048\n"
      "ds_read_b64 v[44:45], v160 offset:1536\n"
      "ds_read_b64 v[46:47], v160 offset:1568\n"
      "ds_read_b64 v[80:81], v163 offset:1536\n"
      "ds_read_b64 v[82:83], v164 offset:1536\n"
      "ds_read_b64 v[48:49], v160 offset:2048\n"
      "ds_read_b64 v[50:51], v160 offset:2080\n"
      "ds_read_b64 v[84:85], v163 offset:1024\n"
      "ds_read_b64 v[86:87], v164 offset:1024\n"
      "ds_read_b64 v[52:53], v160 offset:2560\n"
      "ds_read_b64 v[54:55], v160 offset:2592\n"
      "ds_read_b64 v[88:89], v163 offset:512\n"
      "ds_read_b64 v[90:91], v164 offset:512\n"
      "ds_read_b64 v[56:57], v160 offset:3072\n"
      "ds_read_b64 v[58:59], v160 offset:3104\n"
      "ds_read_b64 v[92:93], v163 offset:0\n"
      "ds_read_b64 v[94:95], v164 offset:0\n"
      "ds_read_b64 v[60:61], v160 offset:3584\n"
      "ds_read_b64 v[62:63], v160 offset:3616\n"
      "v_add_u32 v160, 64, v160\n"
      "v_add_u32 v161, 64, v161\n"
      "v_lshrrev_b32 v165, 3, v161\n"
      "v_and_b32 v165, 32, v165\n"
      "v_add3_u32 v163, v161, v165, v162\n"
      "v_xor_b32 v165, 32, v165\n"
      "v_add3_u32 v164, v161, v165, v162\n"
      "v_and_b32 v8, 3, v250\n"
      "v_lshlrev_b32 v8, 2, v8\n"
      "v_add_u32 v8, 160752, v8\n"
      "s_setprio 0\n"
      "s_mov_b32 s56, 1000000\n"
      ".Lk_go:\n"
      "ds_read_b32 v4, v8\n"
      "s_waitcnt lgkmcnt(0)\n"
      "v_cmp_eq_u32 vcc, 0, v4\n"
      "s_nop 0\n"
      "s_cbranch_vccz .Lk_god\n"
      "s_sleep 2\n"
      "s_sub_u32 s56, s56, 1\n"
      "s_cbranch_scc0 .Lk_go\n"
      ".Lk_god:\n"
      "s_setprio 3\n"
      "v_mfma_f32_16x16x32_bf16 v[0:3], v[168:171], v[64:67], 0\n"
      "ds_read_b64 v[128:129], v163 offset:3584\n"
      "v_mfma_f32_16x16x32_bf16 v[0:3], v[32:35], v[64:67], v[0:3]\n"
      "ds_read_b64 v[130:131], v164 offset:3584\n"
      "v_mfma_f32_16x16x32_bf16 v[4:7], v[168:171], v[68:71], 0\n"
      "ds_read_b64 v[96:97], v160 offset:0\n"
      "v_mfma_f32_16x16x32_bf16 v[4:7], v[36:39], v[64:67], v[4:7]\n"
      "ds_read_b64 v[98:99], v160 offset:32\n"
      "v_mfma_f32_16x16x32_bf16 v[4:7], v[32:35], v[68:71], v[4:7]\n"
      "ds_read_b64 v[132:133], v163 offset:3072\n"
      "v_mfma_f32_16x16x32_bf16 v[8:11], v[168:171], v[72:75], 0\n"
      "ds_read_b64 v[134:135], v164 offset:3072\n"
      "v_mfma_f32_16x16x32_bf16 v[8:11], v[32:35], v[72:75], v[8:11]\n"
      "ds_read_b64 v[100:101], v160 offset:512\n"
      "v_mfma_f32_16x16x32_bf16 v[8:11], v[36:39], v[68:71], v[8:11]\n"
      "ds_read_b64 v[102:103], v160 offset:544\n"
      "v_mfma_f32_16x16x32_bf16 v[8:11], v[40:43], v[64:67], v[8:11]\n"
      "ds_read_b64 v[136:137], v163 offset:2560\n"
      "v_mfma_f32_16x16x32_bf16 v[12:15], v[168:171], v[76:79], 0\n"
      "ds_read_b64 v[138:139], v164 offset:2560\n"
      "v_mfma_f32_16x16x32_bf16 v[12:15], v[44:47], v[64:67], v[12:15]\n"
      "ds_read_b64 v[104:105], v160 offset:1024\n"
      "v_mfma_f32_16x16x32_bf16 v[12:15], v[40:43], v[68:71], v[12:15]\n"
      "ds_read_b64 v[106:107], v160 offset:1056\n"
      "v_mfma_f32_16x16x32_bf16 v[12:15], v[36:39], v[72:75], v[12:15]\n"
      "ds_read_b64 v[140:141], v163 offset:2048\n"
      "v_mfma_f32_16x16x32_bf16 v[12:15], v[32:35], v[76:79], v[12:15]\n"
      "ds_read_b64 v[142:143], v164 offset:2048\n"
      "v_mfma_f32_16x16x32_bf16 v[16:19], v[168:171], v[80:83], 0\n"
      "ds_read_b64 v[108:109], v160 offset:1536\n"
      "v_mfma_f32_16x16x32_bf16 v[16:19], v[32:35], v[80:83], v[16:19]\n"
      "ds_read_b64 v[110:111], v160 offset:1568\n"
      "v_mfma_f32_16x16x32_bf16 v[16:19], v[36:39], v[76:79], v[16:19]\n"
      "ds_read_b64 v[144:145], v163 offset:1536\n"
      "v_mfma_f32_16x16x32_bf16 v[16:19], v[40:43], v[72:75], v[16:19]\n"
      "ds_read_b64 v[146:147], v164 offset:1536\n"
      "v_mfma_f32_16x16x32_bf16 v[16:19], v[44:47], v[68:71], v[16:19]\n"
      "ds_read_b64 v[112:113], v160 offset:2048\n"
      "v_mfma_f32_16x16x32_bf16 v[16:19], v[48:51], v[64:67], v[16:19]\n"
      "ds_read_b64 v[114:115], v160 offset:2080\n"
      "v_mfma_f32_16x16x32_bf16 v[20:23], v[168:171], v[84:87], 0\n"
      "ds_read_b64 v[148:149], v163 offset:1024\n"
      "v_mfma_f32_16x16x32_bf16 v[20:23], v[52:55], v[64:67], v[20:23]\n"
      "ds_read_b64 v[150:151], v164 offset:1024\n"
      "v_mfma_f32_16x16x32_bf16 v[20:23], v[48:51], v[68:71], v[20:23]\n"
      "ds_read_b64 v[116:117], v160 offset:2560\n"
      "v_mfma_f32_16x16x32_bf16 v[20:23], v[44:47], v[72:75], v[20:23]\n"
      "ds_read_b64 v[118:119], v160 offset:2592\n"
      "v_mfma_f32_16x16x32_bf16 v[20:23], v[40:43], v[76:79], v[20:23]\n"
      "ds_read_b64 v[152:153], v163 offset:512\n"
      "v_mfma_f32_16x16x32_bf16 v[20:23], v[36:39], v[80:83], v[20:23]\n"
      "ds_read_b64 v[154:155], v164 offset:512\n"
      "v_mfma_f32_16x16x32_bf16 v[20:23], v[32:35], v[84:87], v[20:23]\n"
      "ds_read_b64 v[120:121], v160 offset:3072\n"
      "v_mfma_f32_16x16x32_bf16 v[24:27], v[168:171], v[88:91], 0\n"
      "ds_read_b64 v[122:123], v160 offset:3104\n"
      "v_mfma_f32_16x16x32_bf16 v[24:27], v[32:35], v[88:91], v[24:27]\n"
      "ds_read_b64 v[156:157], v163 offset:0\n"
      "v_mfma_f32_16x16x32_bf16 v[24:27], v[36:39], v[84:87], v[24:27]\n"
      "ds_read_b64 v[158:159], v164 offset:0\n"
      "v_mfma_f32_16x16x32_bf16 v[24:27], v[40:43], v[80:83], v[24:27]\n"
      "ds_read_b64 v[124:125], v160 offset:3584\n"
      "v_mfma_f32_16x16x32_bf16 v[24:27], v[44:47], v[76:79], v[24:27]\n"
      "ds_read_b64 v[126:127], v160 offset:3616\n"
      "v_mfma_f32_16x16x32_bf16 v[24:27], v[48:51], v[72:75], v[24:27]\n"
      "v_add_u32 v160, 64, v160\n"
      "v_add_u32 v161, 64, v161\n"
      "v_mfma_f32_16x16x32_bf16 v[24:27], v[52:55], v[68:71], v[24:27]\n"
      "v_lshrrev_b32 v165, 3, v161\n"
      "v_and_b32 v165, 32, v165\n"
      "v_mfma_f32_16x16x32_bf16 v[24:27], v[56:59], v[64:67], v[24:27]\n"
      "v_add3_u32 v163, v161, v165, v162\n"
      "v_xor_b32 v165, 32, v165\n"
      "v_mfma_f32_16x16x32_bf16 v[28:31], v[168:171], v[92:95], 0\n"
      "v_add3_u32 v164, v161, v165, v162\n"
      "v_mfma_f32_16x16x32_bf16 v[28:31], v[60:63], v[64:67], v[28:31]\n"
      "v_mfma_f32_16x16x32_bf16 v[28:31], v[56:59], v[68:71], v[28:31]\n"
      "v_mfma_f32_16x16x32_bf16 v[28:31], v[52:55], v[72:75], v[28:31]\n"
      "v_mfma_f32_16x16x32_bf16 v[28:31], v[48:51], v[76:79], v[28:31]\n"
      "v_mfma_f32_16x16x32_bf16 v[28:31], v[44:47], v[80:83], v[28:31]\n"
      "v_mfma_f32_16x16x32_bf16 v[28:31], v[40:43], v[84:87], v[28:31]\n"
      "v_mfma_f32_16x16x32_bf16 v[28:31], v[36:39], v[88:91], v[28:31]\n"
      "v_mfma_f32_16x16x32_bf16 v[28:31], v[32:35], v[92:95], v[28:31]\n"
      "s_waitcnt lgkmcnt(0)\n"
      "v_mfma_f32_16x16x32_bf16 v[0:3], v[96:99], v[128:131], v[0:3]\n"
      "ds_read_b64 v[64:65], v163 offset:3584\n"
      "v_mfma_f32_16x16x32_bf16 v[4:7], v[100:103], v[128:131], v[4:7]\n"
      "ds_read_b64 v[66:67], v164 offset:3584\n"
      "v_mfma_f32_16x16x32_bf16 v[4:7], v[96:99], v[132:135], v[4:7]\n"
      "ds_read_b64 v[32:33], v160 offset:0\n"
      "v_mfma_f32_16x16x32_bf16 v[8:11], v[96:99], v[136:139], v[8:11]\n"
      "ds_read_b64 v[34:35], v160 offset:32\n"
      "v_mfma_f32_16x16x32_bf16 v[8:11], v[100:103], v[132:135], v[8:11]\n"
      "ds_read_b64 v[68:69], v163 offset:3072\n"
      "v_mfma_f32_16x16x32_bf16 v[8:11], v[104:107], v[128:131], v[8:11]\n"
      "ds_read_b64 v[70:71], v164 offset:3072\n"
      "v_mfma_f32_16x16x32_bf16 v[12:15], v[108:111], v[128:131], v[12:15]\n"
      "ds_read_b64 v[36:37], v160 offset:512\n"
      "v_mfma_f32_16x16x32_bf16 v[12:15], v[104:107], v[132:135], v[12:15]\n"
      "ds_read_b64 v[38:39], v160 offset:544\n"
      "v_mfma_f32_16x16x32_bf16 v[12:15], v[100:103], v[136:139], v[12:15]\n"
      "ds_read_b64 v[72:73], v163 offset:2560\n"
      "v_mfma_f32_16x16x32_bf16 v[12:15], v[96:99], v[140:143], v[12:15]\n"
      "ds_read_b64 v[74:75], v164 offset:2560\n"
      "v_mfma_f32_16x16x32_bf16 v[16:19], v[96:99], v[144:147], v[16:19]\n"
      "ds_read_b64 v[40:41], v160 offset:1024\n"
      "v_mfma_f32_16x16x32_bf16 v[16:19], v[100:103], v[140:143], v[16:19]\n"
      "ds_read_b64 v[42:43], v160 offset:1056\n"
      "v_mfma_f32_16x16x32_bf16 v[16:19], v[104:107], v[136:139], v[16:19]\n"
      "ds_read_b64 v[76:77], v163 offset:2048\n"
      "v_mfma_f32_16x16x32_bf16 v[16:19], v[108:111], v[132:135], v[16:19]\n"
      "ds_read_b64 v[78:79], v164 offset:2048\n"
      "v_mfma_f32_16x16x32_bf16 v[16:19], v[112:115], v[128:131], v[16:19]\n"
      "ds_read_b64 v[44:45], v160 offset:1536\n"
      "v_mfma_f32_16x16x32_bf16 v[20:23], v[116:119], v[128:131], v[20:23]\n"
      "ds_read_b64 v[46:47], v160 offset:1568\n"
      "v_mfma_f32_16x16x32_bf16 v[20:23], v[112:115], v[132:135], v[20:23]\n"
      "ds_read_b64 v[80:81], v163 offset:1536\n"
      "v_mfma_f32_16x16x32_bf16 v[20:23], v[108:111], v[136:139], v[20:23]\n"
      "ds_read_b64 v[82:83], v164 offset:1536\n"
      "v_mfma_f32_16x16x32_bf16 v[20:23], v[104:107], v[140:143], v[20:23]\n"
      "ds_read_b64 v[48:49], v160 offset:2048\n"
      "v_mfma_f32_16x16x32_bf16 v[20:23], v[100:103], v[144:147], v[20:23]\n"
      "ds_read_b64 v[50:51], v160 offset:2080\n"
      "v_mfma_f32_16x16x32_bf16 v[20:23], v[96:99], v[148:151], v[20:23]\n"
      "ds_read_b64 v[84:85], v163 offset:1024\n"
      "v_mfma_f32_16x16x32_bf16 v[24:27], v[96:99], v[152:155], v[24:27]\n"
      "ds_read_b64 v[86:87], v164 offset:1024\n"
      "v_mfma_f32_16x16x32_bf16 v[24:27], v[100:103], v[148:151], v[24:27]\n"
      "ds_read_b64 v[52:53], v160 offset:2560\n"
      "v_mfma_f32_16x16x32_bf16 v[24:27], v[104:107], v[144:147], v[24:27]\n"
      "ds_read_b64 v[54:55], v160 offset:2592\n"
      "v_mfma_f32_16x16x32_bf16 v[24:27], v[108:111], v[140:143], v[24:27]\n"
      "ds_read_b64 v[88:89], v163 offset:512\n"
      "v_mfma_f32_16x16x32_bf16 v[24:27], v[112:115], v[136:139], v[24:27]\n"
      "ds_read_b64 v[90:91], v164 offset:512\n"
      "v_mfma_f32_16x16x32_bf16 v[24:27], v[116:119], v[132:135], v[24:27]\n"
      "ds_read_b64 v[56:57], v160 offset:3072\n"
      "v_mfma_f32_16x16x32_bf16 v[24:27], v[120:123], v[128:131], v[24:27]\n"
      "ds_read_b64 v[58:59], v160 offset:3104\n"
      "v_mfma_f32_16x16x32_bf16 v[28:31], v[124:127], v[128:131], v[28:31]\n"
      "ds_read_b64 v[92:93], v163 offset:0\n"
      "v_mfma_f32_16x16x32_bf16 v[28:31], v[120:123], v[132:135], v[28:31]\n"
      "ds_read_b64 v[94:95], v164 offset:0\n"
      "v_mfma_f32_16x16x32_bf16 v[28:31], v[116:119], v[136:139], v[28:31]\n"
      "ds_read_b64 v[60:61], v160 offset:3584\n"
      "v_mfma_f32_16x16x32_bf16 v[28:31], v[112:115], v[140:143], v[28:31]\n"
      "ds_read_b64 v[62:63], v160 offset:3616\n"
      "v_mfma_f32_16x16x32_bf16 v[28:31], v[108:111], v[144:147], v[28:31]\n"
      "v_add_u32 v160, 64, v160\n"
      "v_add_u32 v161, 64, v161\n"
      "v_mfma_f32_16x16x32_bf16 v[28:31], v[104:107], v[148:151], v[28:31]\n"
      "v_lshrrev_b32 v165, 3, v161\n"
      "v_and_b32 v165, 32, v165\n"
      "v_mfma_f32_16x16x32_bf16 v[28:31], v[100:103], v[152:155], v[28:31]\n"
      "v_add3_u32 v163, v161, v165, v162\n"
      "v_xor_b32 v165, 32, v165\n"
      "v_mfma_f32_16x16x32_bf16 v[28:31], v[96:99], v[156:159], v[28:31]\n"
      "v_add3_u32 v164, v161, v165, v162\n"
      "s_sleep 2\n"
      "s_waitcnt lgkmcnt(4)\n"
      "v_mfma_f32_16x16x32_bf16 v[0:3], v[32:35], v[64:67], v[0:3]\n"
      "ds_read_b64 v[128:129], v163 offset:3584\n"
      "v_mfma_f32_16x16x32_bf16 v[4:7], v[36:39], v[64:67], v[4:7]\n"
      "ds_read_b64 v[130:131], v164 offset:3584\n"
      "v_mfma_f32_16x16x32_bf16 v[4:7], v[32:35], v[68:71], v[4:7]\n"
      "ds_read_b64 v[96:97], v160 offset:0\n"
      "v_mfma_f32_16x16x32_bf16 v[8:11], v[32:35], v[72:75], v[8:11]\n"
      "ds_read_b64 v[98:99], v160 offset:32\n"
      "v_mfma_f32_16x16x32_bf16 v[8:11], v[36:39], v[68:71], v[8:11]\n"
      "ds_read_b64 v[132:133], v163 offset:3072\n"
      "v_mfma_f32_16x16x32_bf16 v[8:11], v[40:43], v[64:67], v[8:11]\n"
      "ds_read_b64 v[134:135], v164 offset:3072\n"
      "v_mfma_f32_16x16x32_bf16 v[12:15], v[44:47], v[64:67], v[12:15]\n"
      "ds_read_b64 v[100:101], v160 offset:512\n"
      "v_mfma_f32_16x16x32_bf16 v[12:15], v[40:43], v[68:71], v[12:15]\n"
      "ds_read_b64 v[102:103], v160 offset:544\n"
      "v_mfma_f32_16x16x32_bf16 v[12:15], v[36:39], v[72:75], v[12:15]\n"
      "ds_read_b64 v[136:137], v163 offset:2560\n"
      "v_mfma_f32_16x16x32_bf16 v[12:15], v[32:35], v[76:79], v[12:15]\n"
      "ds_read_b64 v[138:139], v164 offset:2560\n"
      "v_mfma_f32_16x16x32_bf16 v[16:19], v[32:35], v[80:83], v[16:19]\n"
      "ds_read_b64 v[104:105], v160 offset:1024\n"
      "v_mfma_f32_16x16x32_bf16 v[16:19], v[36:39], v[76:79], v[16:19]\n"
      "ds_read_b64 v[106:107], v160 offset:1056\n"
      "v_mfma_f32_16x16x32_bf16 v[16:19], v[40:43], v[72:75], v[16:19]\n"
      "ds_read_b64 v[140:141], v163 offset:2048\n"
      "v_mfma_f32_16x16x32_bf16 v[16:19], v[44:47], v[68:71], v[16:19]\n"
      "ds_read_b64 v[142:143], v164 offset:2048\n"
      "v_mfma_f32_16x16x32_bf16 v[16:19], v[48:51], v[64:67], v[16:19]\n"
      "ds_read_b64 v[108:109], v160 offset:1536\n"
      "v_mfma_f32_16x16x32_bf16 v[20:23], v[52:55], v[64:67], v[20:23]\n"
      "ds_read_b64 v[110:111], v160 offset:1568\n"
      "v_mfma_f32_16x16x32_bf16 v[20:23], v[48:51], v[68:71], v[20:23]\n"
      "ds_read_b64 v[144:145], v163 offset:1536\n"
      "v_mfma_f32_16x16x32_bf16 v[20:23], v[44:47], v[72:75], v[20:23]\n"
      "ds_read_b64 v[146:147], v164 offset:1536\n"
      "v_mfma_f32_16x16x32_bf16 v[20:23], v[40:43], v[76:79], v[20:23]\n"
      "ds_read_b64 v[112:113], v160 offset:2048\n"
      "v_mfma_f32_16x16x32_bf16 v[20:23], v[36:39], v[80:83], v[20:23]\n"
      "ds_read_b64 v[114:115], v160 offset:2080\n"
      "v_mfma_f32_16x16x32_bf16 v[20:23], v[32:35], v[84:87], v[20:23]\n"
      "ds_read_b64 v[148:149], v163 offset:1024\n"
      "v_mfma_f32_16x16x32_bf16 v[24:27], v[32:35], v[88:91], v[24:27]\n"
      "ds_read_b64 v[150:151], v164 offset:1024\n"
      "v_mfma_f32_16x16x32_bf16 v[24:27], v[36:39], v[84:87], v[24:27]\n"
      "ds_read_b64 v[116:117], v160 offset:2560\n"
      "v_mfma_f32_16x16x32_bf16 v[24:27], v[40:43], v[80:83], v[24:27]\n"
      "ds_read_b64 v[118:119], v160 offset:2592\n"
      "v_mfma_f32_16x16x32_bf16 v[24:27], v[44:47], v[76:79], v[24:27]\n"
      "ds_read_b64 v[152:153], v163 offset:512\n"
      "v_mfma_f32_16x16x32_bf16 v[24:27], v[48:51], v[72:75], v[24:27]\n"
      "ds_read_b64 v[154:155], v164 offset:512\n"
      "v_mfma_f32_16x16x32_bf16 v[24:27], v[52:55], v[68:71], v[24:27]\n"
      "ds_read_b64 v[120:121], v160 offset:3072\n"
      "v_mfma_f32_16x16x32_bf16 v[24:27], v[56:59], v[64:67], v[24:27]\n"
      "ds_read_b64 v[122:123], v160 offset:3104\n"
      "s_waitcnt lgkmcnt(8)\n"
      "v_mfma_f32_16x16x32_bf16 v[28:31], v[60:63], v[64:67], v[28:31]\n"
      "ds_read_b64 v[156:157], v163 offset:0\n"
      "v_mfma_f32_16x16x32_bf16 v[28:31], v[56:59], v[68:71], v[28:31]\n"
      "ds_read_b64 v[158:159], v164 offset:0\n"
      "v_mfma_f32_16x16x32_bf16 v[28:31], v[52:55], v[72:75], v[28:31]\n"
      "ds_read_b64 v[124:125], v160 offset:3584\n"
      "v_mfma_f32_16x16x32_bf16 v[28:31], v[48:51], v[76:79], v[28:31]\n"
      "ds_read_b64 v[126:127], v160 offset:3616\n"
      "v_mfma_f32_16x16x32_bf16 v[28:31], v[44:47], v[80:83], v[28:31]\n"
      "v_add_u32 v160, 64, v160\n"
      "v_add_u32 v161, 64, v161\n"
      "v_mfma_f32_16x16x32_bf16 v[28:31], v[40:43], v[84:87], v[28:31]\n"
      "v_lshrrev_b32 v165, 3, v161\n"
      "v_and_b32 v165, 32, v165\n"
      "v_mfma_f32_16x16x32_bf16 v[28:31], v[36:39], v[88:91], v[28:31]\n"
      "v_add3_u32 v163, v161, v165, v162\n"
      "v_xor_b32 v165, 32, v165\n"
      "v_mfma_f32_16x16x32_bf16 v[28:31], v[32:35], v[92:95], v[28:31]\n"
      "v_add3_u32 v164, v161, v165, v162\n"
      "s_sleep 2\n"
      "v_mfma_f32_16x16x32_bf16 v[0:3], v[96:99], v[128:131], v[0:3]\n"
      "ds_read_b64 v[64:65], v163 offset:3584\n"
      "v_mfma_f32_16x16x32_bf16 v[4:7], v[100:103], v[128:131], v[4:7]\n"
      "ds_read_b64 v[66:67], v164 offset:3584\n"
      "v_mfma_f32_16x16x32_bf16 v[4:7], v[96:99], v[132:135], v[4:7]\n"
      "ds_read_b64 v[32:33], v160 offset:0\n"
      "v_mfma_f32_16x16x32_bf16 v[8:11], v[96:99], v[136:139], v[8:11]\n"
      "ds_read_b64 v[34:35], v160 offset:32\n"
      "v_mfma_f32_16x16x32_bf16 v[8:11], v[100:103], v[132:135], v[8:11]\n"
      "ds_read_b64 v[68:69], v163 offset:3072\n"
      "v_mfma_f32_16x16x32_bf16 v[8:11], v[104:107], v[128:131], v[8:11]\n"
      "ds_read_b64 v[70:71], v164 offset:3072\n"
      "v_mfma_f32_16x16x32_bf16 v[12:15], v[108:111], v[128:131], v[12:15]\n"
      "ds_read_b64 v[36:37], v160 offset:512\n"
      "v_mfma_f32_16x16x32_bf16 v[12:15], v[104:107], v[132:135], v[12:15]\n"
      "ds_read_b64 v[38:39], v160 offset:544\n"
      "v_mfma_f32_16x16x32_bf16 v[12:15], v[100:103], v[136:139], v[12:15]\n"
      "ds_read_b64 v[72:73], v163 offset:2560\n"
      "v_mfma_f32_16x16x32_bf16 v[12:15], v[96:99], v[140:143], v[12:15]\n"
      "ds_read_b64 v[74:75], v164 offset:2560\n"
      "v_mfma_f32_16x16x32_bf16 v[16:19], v[96:99], v[144:147], v[16:19]\n"
      "ds_read_b64 v[40:41], v160 offset:1024\n"
      "v_mfma_f32_16x16x32_bf16 v[16:19], v[100:103], v[140:143], v[16:19]\n"
      "ds_read_b64 v[42:43], v160 offset:1056\n"
      "v_mfma_f32_16x16x32_bf16 v[16:19], v[104:107], v[136:139], v[16:19]\n"
      "ds_read_b64 v[76:77], v163 offset:2048\n"
      "v_mfma_f32_16x16x32_bf16 v[16:19], v[108:111], v[132:135], v[16:19]\n"
      "ds_read_b64 v[78:79], v164 offset:2048\n"
      "v_mfma_f32_16x16x32_bf16 v[16:19], v[112:115], v[128:131], v[16:19]\n"
      "ds_read_b64 v[44:45], v160 offset:1536\n"
      "s_waitcnt lgkmcnt(8)\n"
      "v_mfma_f32_16x16x32_bf16 v[20:23], v[116:119], v[128:131], v[20:23]\n"
      "ds_read_b64 v[46:47], v160 offset:1568\n"
      "v_mfma_f32_16x16x32_bf16 v[20:23], v[112:115], v[132:135], v[20:23]\n"
      "ds_read_b64 v[80:81], v163 offset:1536\n"
      "v_mfma_f32_16x16x32_bf16 v[20:23], v[108:111], v[136:139], v[20:23]\n"
      "ds_read_b64 v[82:83], v164 offset:1536\n"
      "v_mfma_f32_16x16x32_bf16 v[20:23], v[104:107], v[140:143], v[20:23]\n"
      "ds_read_b64 v[48:49], v160 offset:2048\n"
      "v_mfma_f32_16x16x32_bf16 v[20:23], v[100:103], v[144:147], v[20:23]\n"
      "ds_read_b64 v[50:51], v160 offset:2080\n"
      "v_mfma_f32_16x16x32_bf16 v[20:23], v[96:99], v[148:151], v[20:23]\n"
      "ds_read_b64 v[84:85], v163 offset:1024\n"
      "v_mfma_f32_16x16x32_bf16 v[24:27], v[96:99], v[152:155], v[24:27]\n"
      "ds_read_b64 v[86:87], v164 offset:1024\n"
      "v_mfma_f32_16x16x32_bf16 v[24:27], v[100:103], v[148:151], v[24:27]\n"
      "ds_read_b64 v[52:53], v160 offset:2560\n"
      "v_mfma_f32_16x16x32_bf16 v[24:27], v[104:107], v[144:147], v[24:27]\n"
      "ds_read_b64 v[54:55], v160 offset:2592\n"
      "v_mfma_f32_16x16x32_bf16 v[24:27], v[108:111], v[140:143], v[24:27]\n"
      "ds_read_b64 v[88:89], v163 offset:512\n"
      "v_mfma_f32_16x16x32_bf16 v[24:27], v[112:115], v[136:139], v[24:27]\n"
      "ds_read_b64 v[90:91], v164 offset:512\n"
      "v_mfma_f32_16x16x32_bf16 v[24:27], v[116:119], v[132:135], v[24:27]\n"
      "ds_read_b64 v[56:57], v160 offset:3072\n"
      "v_mfma_f32_16x16x32_bf16 v[24:27], v[120:123], v[128:131], v[24:27]\n"
      "ds_read_b64 v[58:59], v160 offset:3104\n"
      "v_mfma_f32_16x16x32_bf16 v[28:31], v[124:127], v[128:131], v[28:31]\n"
      "ds_read_b64 v[92:93], v163 offset:0\n"
      "v_mfma_f32_16x16x32_bf16 v[28:31], v[120:123], v[132:135], v[28:31]\n"
      "ds_read_b64 v[94:95], v164 offset:0\n"
      "v_mfma_f32_16x16x32_bf16 v[28:31], v[116:119], v[136:139], v[28:31]\n"
      "ds_read_b64 v[60:61], v160 offset:3584\n"
      "v_mfma_f32_16x16x32_bf16 v[28:31], v[112:115], v[140:143], v[28:31]\n"
      "ds_read_b64 v[62:63], v160 offset:3616\n"
      "v_mfma_f32_16x16x32_bf16 v[28:31], v[108:111], v[144:147], v[28:31]\n"
      "v_add_u32 v160, 64, v160\n"
      "v_add_u32 v161, 64, v161\n"
      "v_mfma_f32_16x16x32_bf16 v[28:31], v[104:107], v[148:151], v[28:31]\n"
      "v_lshrrev_b32 v165, 3, v161\n"
      "v_and_b32 v165, 32, v165\n"
      "v_mfma_f32_16x16x32_bf16 v[28:31], v[100:103], v[152:155], v[28:31]\n"
      "v_add3_u32 v163, v161, v165, v162\n"
      "v_xor_b32 v165, 32, v165\n"
      "v_mfma_f32_16x16x32_bf16 v[28:31], v[96:99], v[156:159], v[28:31]\n"
      "v_add3_u32 v164, v161, v165, v162\n"
      "s_sleep 2\n"
      "v_mfma_f32_16x16x32_bf16 v[0:3], v[32:35], v[64:67], v[0:3]\n"
      "ds_read_b64 v[128:129], v163 offset:3584\n"
      "s_waitcnt lgkmcnt(4)\n"
      "v_mfma_f32_16x16x32_bf16 v[4:7], v[36:39], v[64:67], v[4:7]\n"
      "ds_read_b64 v[130:131], v164 offset:3584\n"
      "v_mfma_f32_16x16x32_bf16 v[4:7], v[32:35], v[68:71], v[4:7]\n"
      "ds_read_b64 v[96:97], v160 offset:0\n"
      "v_mfma_f32_16x16x32_bf16 v[8:11], v[32:35], v[72:75], v[8:11]\n"
      "ds_read_b64 v[98:99], v160 offset:32\n"
      "v_mfma_f32_16x16x32_bf16 v[8:11], v[36:39], v[68:71], v[8:11]\n"
      "ds_read_b64 v[132:133], v163 offset:3072\n"
      "v_mfma_f32_16x16x32_bf16 v[8:11], v[40:43], v[64:67], v[8:11]\n"
      "ds_read_b64 v[134:135], v164 offset:3072\n"
      "v_mfma_f32_16x16x32_bf16 v[12:15], v[44:47], v[64:67], v[12:15]\n"
      "ds_read_b64 v[100:101], v160 offset:512\n"
      "v_mfma_f32_16x16x32_bf16 v[12:15], v[40:43], v[68:71], v[12:15]\n"
      "ds_read_b64 v[102:103], v160 offset:544\n"
      "v_mfma_f32_16x16x32_bf16 v[12:15], v[36:39], v[72:75], v[12:15]\n"
      "ds_read_b64 v[136:137], v163 offset:2560\n"
      "v_mfma_f32_16x16x32_bf16 v[12:15], v[32:35], v[76:79], v[12:15]\n"
      "ds_read_b64 v[138:139], v164 offset:2560\n"
      "v_mfma_f32_16x16x32_bf16 v[16:19], v[32:35], v[80:83], v[16:19]\n"
      "ds_read_b64 v[104:105], v160 offset:1024\n"
      "v_mfma_f32_16x16x32_bf16 v[16:19], v[36:39], v[76:79], v[16:19]\n"
      "ds_read_b64 v[106:107], v160 offset:1056\n"
      "v_mfma_f32_16x16x32_bf16 v[16:19], v[40:43], v[72:75], v[16:19]\n"
      "ds_read_b64 v[140:141], v163 offset:2048\n"
      "v_mfma_f32_16x16x32_bf16 v[16:19], v[44:47], v[68:71], v[16:19]\n"
      "ds_read_b64 v[142:143], v164 offset:2048\n"
      "v_mfma_f32_16x16x32_bf16 v[16:19], v[48:51], v[64:67], v[16:19]\n"
      "ds_read_b64 v[108:109], v160 offset:1536\n"
      "v_mfma_f32_16x16x32_bf16 v[20:23], v[52:55], v[64:67], v[20:23]\n"
      "ds_read_b64 v[110:111], v160 offset:1568\n"
      "v_mfma_f32_16x16x32_bf16 v[20:23], v[48:51], v[68:71], v[20:23]\n"
      "ds_read_b64 v[144:145], v163 offset:1536\n"
      "v_mfma_f32_16x16x32_bf16 v[20:23], v[44:47], v[72:75], v[20:23]\n"
      "ds_read_b64 v[146:147], v164 offset:1536\n"
      "v_mfma_f32_16x16x32_bf16 v[20:23], v[40:43], v[76:79], v[20:23]\n"
      "ds_read_b64 v[112:113], v160 offset:2048\n"
      "v_mfma_f32_16x16x32_bf16 v[20:23], v[36:39], v[80:83], v[20:23]\n"
      "ds_read_b64 v[114:115], v160 offset:2080\n"
      "v_mfma_f32_16x16x32_bf16 v[20:23], v[32:35], v[84:87], v[20:23]\n"
      "ds_read_b64 v[148:149], v163 offset:1024\n"
      "v_mfma_f32_16x16x32_bf16 v[24:27], v[32:35], v[88:91], v[24:27]\n"
      "ds_read_b64 v[150:151], v164 offset:1024\n"
      "v_mfma_f32_16x16x32_bf16 v[24:27], v[36:39], v[84:87], v[24:27]\n"
      "ds_read_b64 v[116:117], v160 offset:2560\n"
      "v_mfma_f32_16x16x32_bf16 v[24:27], v[40:43], v[80:83], v[24:27]\n"
      "ds_read_b64 v[118:119], v160 offset:2592\n"
      "v_mfma_f32_16x16x32_bf16 v[24:27], v[44:47], v[76:79], v[24:27]\n"
      "ds_read_b64 v[152:153], v163 offset:512\n"
      "v_mfma_f32_16x16x32_bf16 v[24:27], v[48:51], v[72:75], v[24:27]\n"
      "ds_read_b64 v[154:155], v164 offset:512\n"
      "v_mfma_f32_16x16x32_bf16 v[24:27], v[52:55], v[68:71], v[24:27]\n"
      "ds_read_b64 v[120:121], v160 offset:3072\n"
      "v_mfma_f32_16x16x32_bf16 v[24:27], v[56:59], v[64:67], v[24:27]\n"
      "ds_read_b64 v[122:123], v160 offset:3104\n"
      "s_waitcnt lgkmcnt(8)\n"
      "v_mfma_f32_16x16x32_bf16 v[28:31], v[60:63], v[64:67], v[28:31]\n"
      "ds_read_b64 v[156:157], v163 offset:0\n"
      "v_mfma_f32_16x16x32_bf16 v[28:31], v[56:59], v[68:71], v[28:31]\n"
      "ds_read_b64 v[158:159], v164 offset:0\n"
      "v_mfma_f32_16x16x32_bf16 v[28:31], v[52:55], v[72:75], v[28:31]\n"
      "ds_read_b64 v[124:125], v160 offset:3584\n"
      "v_mfma_f32_16x16x32_bf16 v[28:31], v[48:51], v[76:79], v[28:31]\n"
      "ds_read_b64 v[126:127], v160 offset:3616\n"
      "v_mfma_f32_16x16x32_bf16 v[28:31], v[44:47], v[80:83], v[28:31]\n"
      "v_add_u32 v160, 64, v160\n"
      "v_add_u32 v161, 64, v161\n"
      "v_mfma_f32_16x16x32_bf16 v[28:31], v[40:43], v[84:87], v[28:31]\n"
      "v_lshrrev_b32 v165, 3, v161\n"
      "v_and_b32 v165, 32, v165\n"
      "v_mfma_f32_16x16x32_bf16 v[28:31], v[36:39], v[88:91], v[28:31]\n"
      "v_add3_u32 v163, v161, v165, v162\n"
      "v_xor_b32 v165, 32, v165\n"
      "v_mfma_f32_16x16x32_bf16 v[28:31], v[32:35], v[92:95], v[28:31]\n"
      "v_add3_u32 v164, v161, v165, v162\n"
      "s_sleep 1\n"
      "v_mfma_f32_16x16x32_bf16 v[0:3], v[96:99], v[128:131], v[0:3]\n"
      "ds_read_b64 v[64:65], v163 offset:3584\n"
      "v_mfma_f32_16x16x32_bf16 v[4:7], v[100:103], v[128:131], v[4:7]\n"
      "ds_read_b64 v[66:67], v164 offset:3584\n"
      "v_mfma_f32_16x16x32_bf16 v[4:7], v[96:99], v[132:135], v[4:7]\n"
      "ds_read_b64 v[32:33], v160 offset:0\n"
      "v_mfma_f32_16x16x32_bf16 v[8:11], v[96:99], v[136:139], v[8:11]\n"
      "ds_read_b64 v[34:35], v160 offset:32\n"
      "v_mfma_f32_16x16x32_bf16 v[8:11], v[100:103], v[132:135], v[8:11]\n"
      "ds_read_b64 v[68:69], v163 offset:3072\n"
      "v_mfma_f32_16x16x32_bf16 v[8:11], v[104:107], v[128:131], v[8:11]\n"
      "ds_read_b64 v[70:71], v164 offset:3072\n"
      "v_mfma_f32_16x16x32_bf16 v[12:15], v[108:111], v[128:131], v[12:15]\n"
      "ds_read_b64 v[36:37], v160 offset:512\n"
      "v_mfma_f32_16x16x32_bf16 v[12:15], v[104:107], v[132:135], v[12:15]\n"
      "ds_read_b64 v[38:39], v160 offset:544\n"
      "v_mfma_f32_16x16x32_bf16 v[12:15], v[100:103], v[136:139], v[12:15]\n"
      "ds_read_b64 v[72:73], v163 offset:2560\n"
      "v_mfma_f32_16x16x32_bf16 v[12:15], v[96:99], v[140:143], v[12:15]\n"
      "ds_read_b64 v[74:75], v164 offset:2560\n"
      "v_mfma_f32_16x16x32_bf16 v[16:19], v[96:99], v[144:147], v[16:19]\n"
      "ds_read_b64 v[40:41], v160 offset:1024\n"
      "v_mfma_f32_16x16x32_bf16 v[16:19], v[100:103], v[140:143], v[16:19]\n"
      "ds_read_b64 v[42:43], v160 offset:1056\n"
      "v_mfma_f32_16x16x32_bf16 v[16:19], v[104:107], v[136:139], v[16:19]\n"
      "ds_read_b64 v[76:77], v163 offset:2048\n"
      "v_mfma_f32_16x16x32_bf16 v[16:19], v[108:111], v[132:135], v[16:19]\n"
      "ds_read_b64 v[78:79], v164 offset:2048\n"
      "v_mfma_f32_16x16x32_bf16 v[16:19], v[112:115], v[128:131], v[16:19]\n"
      "ds_read_b64 v[44:45], v160 offset:1536\n"
      "s_waitcnt lgkmcnt(8)\n"
      "v_mfma_f32_16x16x32_bf16 v[20:23], v[116:119], v[128:131], v[20:23]\n"
      "ds_read_b64 v[46:47], v160 offset:1568\n"
      "v_mfma_f32_16x16x32_bf16 v[20:23], v[112:115], v[132:135], v[20:23]\n"
      "ds_read_b64 v[80:81], v163 offset:1536\n"
      "v_mfma_f32_16x16x32_bf16 v[20:23], v[108:111], v[136:139], v[20:23]\n"
      "ds_read_b64 v[82:83], v164 offset:1536\n"
      "v_mfma_f32_16x16x32_bf16 v[20:23], v[104:107], v[140:143], v[20:23]\n"
      "ds_read_b64 v[48:49], v160 offset:2048\n"
      "v_mfma_f32_16x16x32_bf16 v[20:23], v[100:103], v[144:147], v[20:23]\n"
      "ds_read_b64 v[50:51], v160 offset:2080\n"
      "v_mfma_f32_16x16x32_bf16 v[20:23], v[96:99], v[148:151], v[20:23]\n"
      "ds_read_b64 v[84:85], v163 offset:1024\n"
      "v_mfma_f32_16x16x32_bf16 v[24:27], v[96:99], v[152:155], v[24:27]\n"
      "ds_read_b64 v[86:87], v164 offset:1024\n"
      "v_mfma_f32_16x16x32_bf16 v[24:27], v[100:103], v[148:151], v[24:27]\n"
      "ds_read_b64 v[52:53], v160 offset:2560\n"
      "v_mfma_f32_16x16x32_bf16 v[24:27], v[104:107], v[144:147], v[24:27]\n"
      "ds_read_b64 v[54:55], v160 offset:2592\n"
      "v_mfma_f32_16x16x32_bf16 v[24:27], v[108:111], v[140:143], v[24:27]\n"
      "ds_read_b64 v[88:89], v163 offset:512\n"
      "v_mfma_f32_16x16x32_bf16 v[24:27], v[112:115], v[136:139], v[24:27]\n"
      "ds_read_b64 v[90:91], v164 offset:512\n"
      "v_mfma_f32_16x16x32_bf16 v[24:27], v[116:119], v[132:135], v[24:27]\n"
      "ds_read_b64 v[56:57], v160 offset:3072\n"
      "v_mfma_f32_16x16x32_bf16 v[24:27], v[120:123], v[128:131], v[24:27]\n"
      "ds_read_b64 v[58:59], v160 offset:3104\n"
      "v_mfma_f32_16x16x32_bf16 v[28:31], v[124:127], v[128:131], v[28:31]\n"
      "ds_read_b64 v[92:93], v163 offset:0\n"
      "v_mfma_f32_16x16x32_bf16 v[28:31], v[120:123], v[132:135], v[28:31]\n"
      "ds_read_b64 v[94:95], v164 offset:0\n"
      "v_mfma_f32_16x16x32_bf16 v[28:31], v[116:119], v[136:139], v[28:31]\n"
      "ds_read_b64 v[60:61], v160 offset:3584\n"
      "v_mfma_f32_16x16x32_bf16 v[28:31], v[112:115], v[140:143], v[28:31]\n"
      "ds_read_b64 v[62:63], v160 offset:3616\n"
      "v_mfma_f32_16x16x32_bf16 v[28:31], v[108:111], v[144:147], v[28:31]\n"
      "v_add_u32 v160, 64, v160\n"
      "v_add_u32 v161, 64, v161\n"
      "v_mfma_f32_16x16x32_bf16 v[28:31], v[104:107], v[148:151], v[28:31]\n"
      "v_lshrrev_b32 v165, 3, v161\n"
      "v_and_b32 v165, 32, v165\n"
      "v_mfma_f32_16x16x32_bf16 v[28:31], v[100:103], v[152:155], v[28:31]\n"
      "v_add3_u32 v163, v161, v165, v162\n"
      "v_xor_b32 v165, 32, v165\n"
      "v_mfma_f32_16x16x32_bf16 v[28:31], v[96:99], v[156:159], v[28:31]\n"
      "v_add3_u32 v164, v161, v165, v162\n"
      "v_mfma_f32_16x16x32_bf16 v[0:3], v[32:35], v[64:67], v[0:3]\n"
      "ds_read_b64 v[128:129], v163 offset:3584\n"
      "s_waitcnt lgkmcnt(4)\n"
      "v_mfma_f32_16x16x32_bf16 v[4:7], v[36:39], v[64:67], v[4:7]\n"
      "ds_read_b64 v[130:131], v164 offset:3584\n"
      "v_mfma_f32_16x16x32_bf16 v[4:7], v[32:35], v[68:71], v[4:7]\n"
      "ds_read_b64 v[96:97], v160 offset:0\n"
      "v_mfma_f32_16x16x32_bf16 v[8:11], v[32:35], v[72:75], v[8:11]\n"
      "ds_read_b64 v[98:99], v160 offset:32\n"
      "v_mfma_f32_16x16x32_bf16 v[8:11], v[36:39], v[68:71], v[8:11]\n"
      "ds_read_b64 v[132:133], v163 offset:3072\n"
      "v_mfma_f32_16x16x32_bf16 v[8:11], v[40:43], v[64:67], v[8:11]\n"
      "ds_read_b64 v[134:135], v164 offset:3072\n"
      "v_mfma_f32_16x16x32_bf16 v[12:15], v[44:47], v[64:67], v[12:15]\n"
      "ds_read_b64 v[100:101], v160 offset:512\n"
      "v_mfma_f32_16x16x32_bf16 v[12:15], v[40:43], v[68:71], v[12:15]\n"
      "ds_read_b64 v[102:103], v160 offset:544\n"
      "v_mfma_f32_16x16x32_bf16 v[12:15], v[36:39], v[72:75], v[12:15]\n"
      "ds_read_b64 v[136:137], v163 offset:2560\n"
      "v_mfma_f32_16x16x32_bf16 v[12:15], v[32:35], v[76:79], v[12:15]\n"
      "ds_read_b64 v[138:139], v164 offset:2560\n"
      "v_mfma_f32_16x16x32_bf16 v[16:19], v[32:35], v[80:83], v[16:19]\n"
      "ds_read_b64 v[104:105], v160 offset:1024\n"
      "v_mfma_f32_16x16x32_bf16 v[16:19], v[36:39], v[76:79], v[16:19]\n"
      "ds_read_b64 v[106:107], v160 offset:1056\n"
      "v_mfma_f32_16x16x32_bf16 v[16:19], v[40:43], v[72:75], v[16:19]\n"
      "ds_read_b64 v[140:141], v163 offset:2048\n"
      "v_mfma_f32_16x16x32_bf16 v[16:19], v[44:47], v[68:71], v[16:19]\n"
      "ds_read_b64 v[142:143], v164 offset:2048\n"
      "v_mfma_f32_16x16x32_bf16 v[16:19], v[48:51], v[64:67], v[16:19]\n"
      "ds_read_b64 v[108:109], v160 offset:1536\n"
      "v_mfma_f32_16x16x32_bf16 v[20:23], v[52:55], v[64:67], v[20:23]\n"
      "ds_read_b64 v[110:111], v160 offset:1568\n"
      "v_mfma_f32_16x16x32_bf16 v[20:23], v[48:51], v[68:71], v[20:23]\n"
      "ds_read_b64 v[144:145], v163 offset:1536\n"
      "v_mfma_f32_16x16x32_bf16 v[20:23], v[44:47], v[72:75], v[20:23]\n"
      "ds_read_b64 v[146:147], v164 offset:1536\n"
      "v_mfma_f32_16x16x32_bf16 v[20:23], v[40:43], v[76:79], v[20:23]\n"
      "ds_read_b64 v[112:113], v160 offset:2048\n"
      "v_mfma_f32_16x16x32_bf16 v[20:23], v[36:39], v[80:83], v[20:23]\n"
      "ds_read_b64 v[114:115], v160 offset:2080\n"
      "v_mfma_f32_16x16x32_bf16 v[20:23], v[32:35], v[84:87], v[20:23]\n"
      "ds_read_b64 v[148:149], v163 offset:1024\n"
      "v_mfma_f32_16x16x32_bf16 v[24:27], v[32:35], v[88:91], v[24:27]\n"
      "ds_read_b64 v[150:151], v164 offset:1024\n"
      "v_mfma_f32_16x16x32_bf16 v[24:27], v[36:39], v[84:87], v[24:27]\n"
      "ds_read_b64 v[116:117], v160 offset:2560\n"
      "v_mfma_f32_16x16x32_bf16 v[24:27], v[40:43], v[80:83], v[24:27]\n"
      "ds_read_b64 v[118:119], v160 offset:2592\n"
      "v_mfma_f32_16x16x32_bf16 v[24:27], v[44:47], v[76:79], v[24:27]\n"
      "ds_read_b64 v[152:153], v163 offset:512\n"
      "v_mfma_f32_16x16x32_bf16 v[24:27], v[48:51], v[72:75], v[24:27]\n"
      "ds_read_b64 v[154:155], v164 offset:512\n"
      "v_mfma_f32_16x16x32_bf16 v[24:27], v[52:55], v[68:71], v[24:27]\n"
      "ds_read_b64 v[120:121], v160 offset:3072\n"
      "v_mfma_f32_16x16x32_bf16 v[24:27], v[56:59], v[64:67], v[24:27]\n"
      "ds_read_b64 v[122:123], v160 offset:3104\n"
      "s_waitcnt lgkmcnt(8)\n"
      "v_mfma_f32_16x16x32_bf16 v[28:31], v[60:63], v[64:67], v[28:31]\n"
      "ds_read_b64 v[156:157], v163 offset:0\n"
      "v_mfma_f32_16x16x32_bf16 v[28:31], v[56:59], v[68:71], v[28:31]\n"
      "ds_read_b64 v[158:159], v164 offset:0\n"
      "v_mfma_f32_16x16x32_bf16 v[28:31], v[52:55], v[72:75], v[28:31]\n"
      "ds_read_b64 v[124:125], v160 offset:3584\n"
      "v_mfma_f32_16x16x32_bf16 v[28:31], v[48:51], v[76:79], v[28:31]\n"
      "ds_read_b64 v[126:127], v160 offset:3616\n"
      "v_mfma_f32_16x16x32_bf16 v[28:31], v[44:47], v[80:83], v[28:31]\n"
      "v_add_u32 v160, 64, v160\n"
      "v_add_u32 v161, 64, v161\n"
      "v_mfma_f32_16x16x32_bf16 v[28:31], v[40:43], v[84:87], v[28:31]\n"
      "v_lshrrev_b32 v165, 3, v161\n"
      "v_and_b32 v165, 32, v165\n"
      "v_mfma_f32_16x16x32_bf16 v[28:31], v[36:39], v[88:91], v[28:31]\n"
      "v_add3_u32 v163, v161, v165, v162\n"
      "v_xor_b32 v165, 32, v165\n"
      "v_mfma_f32_16x16x32_bf16 v[28:31], v[32:35], v[92:95], v[28:31]\n"
      "v_add3_u32 v164, v161, v165, v162\n"
      "v_mfma_f32_16x16x32_bf16 v[0:3], v[96:99], v[128:131], v[0:3]\n"
      "v_mfma_f32_16x16x32_bf16 v[4:7], v[100:103], v[128:131], v[4:7]\n"
      "v_mfma_f32_16x16x32_bf16 v[4:7], v[96:99], v[132:135], v[4:7]\n"
      "v_mfma_f32_16x16x32_bf16 v[8:11], v[96:99], v[136:139], v[8:11]\n"
      "v_mfma_f32_16x16x32_bf16 v[8:11], v[100:103], v[132:135], v[8:11]\n"
      "s_waitcnt vmcnt(0)\n"
      "s_waitcnt lgkmcnt(0)\n"
      "s_barrier\n"
      "v_mfma_f32_16x16x32_bf16 v[8:11], v[104:107], v[128:131], v[8:11]\n"
      "ds_read_b64_tr_b16 v[218:219], v194 offset:0\n"
      "v_mfma_f32_16x16x32_bf16 v[12:15], v[108:111], v[128:131], v[12:15]\n"
      "ds_read_b64_tr_b16 v[220:221], v194 offset:4128\n"
      "v_mfma_f32_16x16x32_bf16 v[12:15], v[104:107], v[132:135], v[12:15]\n"
      "ds_read_b128 v[80:83], v193 offset:16512\n"
      "v_cvt_pk_bf16_f32 v200, v0, v1\n"
      "v_mfma_f32_16x16x32_bf16 v[12:15], v[100:103], v[136:139], v[12:15]\n"
      "ds_read_b64_tr_b16 v[222:223], v194 offset:8256\n"
      "v_cvt_pk_bf16_f32 v201, v2, v3\n"
      "v_mfma_f32_16x16x32_bf16 v[12:15], v[96:99], v[140:143], v[12:15]\n"
      "ds_read_b64_tr_b16 v[224:225], v194 offset:12384\n"
      "v_cvt_pk_bf16_f32 v202, v4, v5\n"
      "v_mfma_f32_16x16x32_bf16 v[16:19], v[96:99], v[144:147], v[16:19]\n"
      "ds_read_b128 v[84:87], v193 offset:24704\n"
      "v_cvt_pk_bf16_f32 v203, v6, v7\n"
      "v_mfma_f32_16x16x32_bf16 v[16:19], v[100:103], v[140:143], v[16:19]\n"
      "ds_read_b128 v[92:95], v193 offset:25728\n"
      "s_nop 0\n"
      "v_mfma_f32_16x16x32_bf16 v[16:19], v[104:107], v[136:139], v[16:19]\n"
      "ds_read_b128 v[88:91], v193 offset:17536\n"
      "v_permlane32_swap_b32 v200, v202\n"
      "v_mfma_f32_16x16x32_bf16 v[16:19], v[108:111], v[132:135], v[16:19]\n"
      "ds_read_b64_tr_b16 v[226:227], v194 offset:8\n"
      "v_permlane32_swap_b32 v201, v203\n"
      "v_mfma_f32_16x16x32_bf16 v[16:19], v[112:115], v[128:131], v[16:19]\n"
      "ds_read_b64_tr_b16 v[228:229], v194 offset:4136\n"
      "buffer_store_dwordx4 v[200:203], v212, s[32:35], s68 offen offset:0 sc1\n"
      "v_mfma_f32_16x16x32_bf16 v[20:23], v[116:119], v[128:131], v[20:23]\n"
      "ds_read_b64_tr_b16 v[230:231], v194 offset:8264\n"
      "v_cvt_pk_bf16_f32 v204, v8, v9\n"
      "v_mfma_f32_16x16x32_bf16 v[20:23], v[112:115], v[132:135], v[20:23]\n"
      "ds_read_b64_tr_b16 v[232:233], v194 offset:12392\n"
      "v_cvt_pk_bf16_f32 v205, v10, v11\n"
      "v_mfma_f32_16x16x32_bf16 v[20:23], v[108:111], v[136:139], v[20:23]\n"
      "ds_read_b64_tr_b16 v[234:235], v195 offset:0\n"
      "v_cvt_pk_bf16_f32 v206, v12, v13\n"
      "v_mfma_f32_16x16x32_bf16 v[20:23], v[104:107], v[140:143], v[20:23]\n"
      "ds_read_b64_tr_b16 v[236:237], v195 offset:4128\n"
      "v_cvt_pk_bf16_f32 v207, v14, v15\n"
      "v_mfma_f32_16x16x32_bf16 v[20:23], v[100:103], v[144:147], v[20:23]\n"
      "ds_read_b64_tr_b16 v[238:239], v195 offset:8256\n"
      "s_nop 0\n"
      "v_mfma_f32_16x16x32_bf16 v[20:23], v[96:99], v[148:151], v[20:23]\n"
      "ds_read_b64_tr_b16 v[240:241], v195 offset:12384\n"
      "v_permlane32_swap_b32 v204, v206\n"
      "v_mfma_f32_16x16x32_bf16 v[24:27], v[96:99], v[152:155], v[24:27]\n"
      "ds_read_b64_tr_b16 v[242:243], v195 offset:8\n"
      "v_permlane32_swap_b32 v205, v207\n"
      "v_mfma_f32_16x16x32_bf16 v[24:27], v[100:103], v[148:151], v[24:27]\n"
      "ds_read_b64_tr_b16 v[244:245], v195 offset:4136\n"
      "buffer_store_dwordx4 v[204:207], v212, s[32:35], s68 offen offset:1024 sc1\n"
      "v_mfma_f32_16x16x32_bf16 v[24:27], v[104:107], v[144:147], v[24:27]\n"
      "ds_read_b64_tr_b16 v[246:247], v195 offset:8264\n"
      "v_mfma_f32_16x16x32_bf16 v[24:27], v[108:111], v[140:143], v[24:27]\n"
      "ds_read_b64_tr_b16 v[248:249], v195 offset:12392\n"
      "v_mfma_f32_16x16x32_bf16 v[24:27], v[112:115], v[136:139], v[24:27]\n"
      "ds_read_b128 v[64:67], v192 offset:16512\n"
      "v_mfma_f32_16x16x32_bf16 v[24:27], v[116:119], v[132:135], v[24:27]\n"
      "ds_read_b128 v[68:71], v192 offset:24704\n"
      "v_cvt_pk_bf16_f32 v200, v16, v17\n"
      "v_mfma_f32_16x16x32_bf16 v[24:27], v[120:123], v[128:131], v[24:27]\n"
      "ds_read_b128 v[76:79], v192 offset:25728\n"
      "v_cvt_pk_bf16_f32 v201, v18, v19\n"
      "v_mfma_f32_16x16x32_bf16 v[28:31], v[124:127], v[128:131], v[28:31]\n"
      "ds_read_b128 v[72:75], v192 offset:17536\n"
      "v_cvt_pk_bf16_f32 v202, v20, v21\n"
      "v_mfma_f32_16x16x32_bf16 v[28:31], v[120:123], v[132:135], v[28:31]\n"
      "v_cvt_pk_bf16_f32 v203, v22, v23\n"
      "v_mfma_f32_16x16x32_bf16 v[28:31], v[116:119], v[136:139], v[28:31]\n"
      "s_nop 0\n"
      "v_mfma_f32_16x16x32_bf16 v[28:31], v[112:115], v[140:143], v[28:31]\n"
      "v_permlane32_swap_b32 v200, v202\n"
      "v_mfma_f32_16x16x32_bf16 v[28:31], v[108:111], v[144:147], v[28:31]\n"
      "v_add_u32 v192, s63, v192\n"
      "v_permlane32_swap_b32 v201, v203\n"
      "v_mfma_f32_16x16x32_bf16 v[28:31], v[104:107], v[148:151], v[28:31]\n"
      "v_add_u32 v193, s63, v193\n"
      "buffer_store_dwordx4 v[200:203], v212, s[32:35], s68 offen offset:2048 sc1\n"
      "v_mfma_f32_16x16x32_bf16 v[28:31], v[100:103], v[152:155], v[28:31]\n"
      "v_add_u32 v194, s63, v194\n"
      "v_mfma_f32_16x16x32_bf16 v[28:31], v[96:99], v[156:159], v[28:31]\n"
      "v_add_u32 v195, s63, v195\n"
      "s_waitcnt lgkmcnt(0)\n"
      "v_mfma_f32_16x16x32_bf16 v[32:35], v[218:221], v[80:83], 0\n"
      "s_barrier\n"
      "v_mfma_f32_16x16x32_bf16 v[32:35], v[222:225], v[84:87], v[32:35]\n"
      "ds_read_b64_tr_b16 v[96:97], v194 offset:0\n"
      "v_mfma_f32_16x16x32_bf16 v[48:51], v[222:225], v[92:95], 0\n"
      "ds_read_b64_tr_b16 v[98:99], v194 offset:4128\n"
      "v_mfma_f32_16x16x32_bf16 v[48:51], v[218:221], v[88:91], v[48:51]\n"
      "ds_read_b128 v[128:131], v192 offset:16512\n"
      "v_mfma_f32_16x16x32_bf16 v[52:55], v[226:229], v[88:91], 0\n"
      "ds_read_b64_tr_b16 v[100:101], v194 offset:8256\n"
      "v_cvt_pk_bf16_f32 v204, v24, v25\n"
      "v_mfma_f32_16x16x32_bf16 v[52:55], v[230:233], v[92:95], v[52:55]\n"
      "ds_read_b64_tr_b16 v[102:103], v194 offset:12384\n"
      "v_cvt_pk_bf16_f32 v205, v26, v27\n"
      "v_mfma_f32_16x16x32_bf16 v[36:39], v[230:233], v[84:87], 0\n"
      "ds_read_b128 v[132:135], v192 offset:24704\n"
      "v_cvt_pk_bf16_f32 v206, v28, v29\n"
      "v_mfma_f32_16x16x32_bf16 v[36:39], v[226:229], v[80:83], v[36:39]\n"
      "ds_read_b128 v[140:143], v192 offset:25728\n"
      "v_cvt_pk_bf16_f32 v207, v30, v31\n"
      "v_mfma_f32_16x16x32_bf16 v[40:43], v[234:237], v[80:83], 0\n"
      "ds_read_b128 v[136:139], v192 offset:17536\n"
      "s_nop 0\n"
      "v_mfma_f32_16x16x32_bf16 v[40:43], v[238:241], v[84:87], v[40:43]\n"
      "ds_read_b128 v[144:147], v193 offset:16512\n"
      "v_permlane32_swap_b32 v204, v206\n"
      "v_mfma_f32_16x16x32_bf16 v[56:59], v[238:241], v[92:95], 0\n"
      "ds_read_b128 v[148:151], v193 offset:24704\n"
      "v_permlane32_swap_b32 v205, v207\n"
      "v_mfma_f32_16x16x32_bf16 v[56:59], v[234:237], v[88:91], v[56:59]\n"
      "ds_read_b128 v[156:159], v193 offset:25728\n"
      "buffer_store_dwordx4 v[204:207], v212, s[32:35], s68 offen offset:3072 sc1\n"
      "v_mfma_f32_16x16x32_bf16 v[60:63], v[242:245], v[88:91], 0\n"
      "ds_read_b128 v[152:155], v193 offset:17536\n"
      "v_mfma_f32_16x16x32_bf16 v[60:63], v[246:249], v[92:95], v[60:63]\n"
      "ds_read_b64_tr_b16 v[104:105], v194 offset:8\n"
      "v_mfma_f32_16x16x32_bf16 v[44:47], v[246:249], v[84:87], 0\n"
      "ds_read_b64_tr_b16 v[106:107], v194 offset:4136\n"
      "v_mfma_f32_16x16x32_bf16 v[44:47], v[242:245], v[80:83], v[44:47]\n"
      "ds_read_b64_tr_b16 v[108:109], v194 offset:8264\n"
      "v_mfma_f32_16x16x32_bf16 v[12:15], v[242:245], v[64:67], 0\n"
      "ds_read_b64_tr_b16 v[110:111], v194 offset:12392\n"
      "v_mfma_f32_16x16x32_bf16 v[12:15], v[246:249], v[68:71], v[12:15]\n"
      "ds_read_b64_tr_b16 v[112:113], v195 offset:0\n"
      "v_mfma_f32_16x16x32_bf16 v[28:31], v[246:249], v[76:79], 0\n"
      "ds_read_b64_tr_b16 v[114:115], v195 offset:4128\n"
      "v_mfma_f32_16x16x32_bf16 v[28:31], v[242:245], v[72:75], v[28:31]\n"
      "ds_read_b64_tr_b16 v[116:117], v195 offset:8256\n"
      "v_mfma_f32_16x16x32_bf16 v[24:27], v[234:237], v[72:75], 0\n"
      "ds_read_b64_tr_b16 v[118:119], v195 offset:12384\n"
      "s_add_u32 s61, s61, 1\n"
      "v_mfma_f32_16x16x32_bf16 v[24:27], v[238:241], v[76:79], v[24:27]\n"
      "ds_read_b64_tr_b16 v[120:121], v195 offset:8\n"
      "s_cmp_eq_u32 s61, 3\n"
      "v_mfma_f32_16x16x32_bf16 v[8:11], v[238:241], v[68:71], 0\n"
      "ds_read_b64_tr_b16 v[122:123], v195 offset:4136\n"
      "s_cselect_b32 s62, s64, s63\n"
      "v_mfma_f32_16x16x32_bf16 v[8:11], v[234:237], v[64:67], v[8:11]\n"
      "ds_read_b64_tr_b16 v[124:125], v195 offset:8264\n"
      "s_cselect_b32 s61, 0, s61\n"
      "v_mfma_f32_16x16x32_bf16 v[4:7], v[226:229], v[64:67], 0\n"
      "ds_read_b64_tr_b16 v[126:127], v195 offset:12392\n"
      "v_mfma_f32_16x16x32_bf16 v[4:7], v[230:233], v[68:71], v[4:7]\n"
      "v_add_u32 v192, s62, v192\n"
      "v_mfma_f32_16x16x32_bf16 v[20:23], v[230:233], v[76:79], 0\n"
      "v_add_u32 v193, s62, v193\n"
      "v_mfma_f32_16x16x32_bf16 v[20:23], v[226:229], v[72:75], v[20:23]\n"
      "v_add_u32 v194, s62, v194\n"
      "v_mfma_f32_16x16x32_bf16 v[16:19], v[218:221], v[72:75], 0\n"
      "v_add_u32 v195, s62, v195\n"
      "v_mfma_f32_16x16x32_bf16 v[16:19], v[222:225], v[76:79], v[16:19]\n"
      "v_mfma_f32_16x16x32_bf16 v[0:3], v[222:225], v[68:71], 0\n"
      "v_mfma_f32_16x16x32_bf16 v[0:3], v[218:221], v[64:67], v[0:3]\n"
      ".Lk_g2:\n"
      "s_waitcnt lgkmcnt(0)\n"
      "v_mfma_f32_16x16x32_bf16 v[0:3], v[96:99], v[128:131], v[0:3]\n"
      "s_barrier\n"
      "v_mfma_f32_16x16x32_bf16 v[0:3], v[100:103], v[132:135], v[0:3]\n"
      "ds_read_b64_tr_b16 v[218:219], v194 offset:0\n"
      "v_mfma_f32_16x16x32_bf16 v[16:19], v[100:103], v[140:143], v[16:19]\n"
      "ds_read_b64_tr_b16 v[220:221], v194 offset:4128\n"
      "v_mfma_f32_16x16x32_bf16 v[16:19], v[96:99], v[136:139], v[16:19]\n"
      "ds_read_b128 v[64:67], v192 offset:16512\n"
      "v_mfma_f32_16x16x32_bf16 v[32:35], v[96:99], v[144:147], v[32:35]\n"
      "ds_read_b64_tr_b16 v[222:223], v194 offset:8256\n"
      "v_mfma_f32_16x16x32_bf16 v[32:35], v[100:103], v[148:151], v[32:35]\n"
      "ds_read_b64_tr_b16 v[224:225], v194 offset:12384\n"
      "v_mfma_f32_16x16x32_bf16 v[48:51], v[100:103], v[156:159], v[48:51]\n"
      "ds_read_b128 v[68:71], v192 offset:24704\n"
      "v_mfma_f32_16x16x32_bf16 v[48:51], v[96:99], v[152:155], v[48:51]\n"
      "ds_read_b128 v[76:79], v192 offset:25728\n"
      "v_mfma_f32_16x16x32_bf16 v[52:55], v[104:107], v[152:155], v[52:55]\n"
      "ds_read_b128 v[72:75], v192 offset:17536\n"
      "v_mfma_f32_16x16x32_bf16 v[52:55], v[108:111], v[156:159], v[52:55]\n"
      "ds_read_b128 v[80:83], v193 offset:16512\n"
      "v_mfma_f32_16x16x32_bf16 v[36:39], v[108:111], v[148:151], v[36:39]\n"
      "ds_read_b128 v[84:87], v193 offset:24704\n"
      "v_mfma_f32_16x16x32_bf16 v[36:39], v[104:107], v[144:147], v[36:39]\n"
      "ds_read_b128 v[92:95], v193 offset:25728\n"
      "v_mfma_f32_16x16x32_bf16 v[20:23], v[104:107], v[136:139], v[20:23]\n"
      "ds_read_b128 v[88:91], v193 offset:17536\n"
      "v_mfma_f32_16x16x32_bf16 v[20:23], v[108:111], v[140:143], v[20:23]\n"
      "ds_read_b64_tr_b16 v[226:227], v194 offset:8\n"
      "v_mfma_f32_16x16x32_bf16 v[4:7], v[108:111], v[132:135], v[4:7]\n"
      "ds_read_b64_tr_b16 v[228:229], v194 offset:4136\n"
      "v_mfma_f32_16x16x32_bf16 v[4:7], v[104:107], v[128:131], v[4:7]\n"
      "ds_read_b64_tr_b16 v[230:231], v194 offset:8264\n"
      "v_mfma_f32_16x16x32_bf16 v[8:11], v[112:115], v[128:131], v[8:11]\n"
      "ds_read_b64_tr_b16 v[232:233], v194 offset:12392\n"
      "v_mfma_f32_16x16x32_bf16 v[8:11], v[116:119], v[132:135], v[8:11]\n"
      "ds_read_b64_tr_b16 v[234:235], v195 offset:0\n"
      "v_mfma_f32_16x16x32_bf16 v[24:27], v[116:119], v[140:143], v[24:27]\n"
      "ds_read_b64_tr_b16 v[236:237], v195 offset:4128\n"
      "v_mfma_f32_16x16x32_bf16 v[24:27], v[112:115], v[136:139], v[24:27]\n"
      "ds_read_b64_tr_b16 v[238:239], v195 offset:8256\n"
      "v_mfma_f32_16x16x32_bf16 v[40:43], v[112:115], v[144:147], v[40:43]\n"
      "ds_read_b64_tr_b16 v[240:241], v195 offset:12384\n"
      "s_add_u32 s61, s61, 1\n"
      "v_mfma_f32_16x16x32_bf16 v[40:43], v[116:119], v[148:151], v[40:43]\n"
      "ds_read_b64_tr_b16 v[242:243], v195 offset:8\n"
      "s_cmp_eq_u32 s61, 3\n"
      "v_mfma_f32_16x16x32_bf16 v[56:59], v[116:119], v[156:159], v[56:59]\n"
      "ds_read_b64_tr_b16 v[244:245], v195 offset:4136\n"
      "s_cselect_b32 s62, s64, s63\n"
      "v_mfma_f32_16x16x32_bf16 v[56:59], v[112:115], v[152:155], v[56:59]\n"
      "ds_read_b64_tr_b16 v[246:247], v195 offset:8264\n"
      "s_cselect_b32 s61, 0, s61\n"
      "v_mfma_f32_16x16x32_bf16 v[60:63], v[120:123], v[152:155], v[60:63]\n"
      "ds_read_b64_tr_b16 v[248:249], v195 offset:12392\n"
      "v_mfma_f32_16x16x32_bf16 v[60:63], v[124:127], v[156:159], v[60:63]\n"
      "v_add_u32 v192, s62, v192\n"
      "v_mfma_f32_16x16x32_bf16 v[44:47], v[124:127], v[148:151], v[44:47]\n"
      "v_add_u32 v193, s62, v193\n"
      "v_mfma_f32_16x16x32_bf16 v[44:47], v[120:123], v[144:147], v[44:47]\n"
      "v_add_u32 v194, s62, v194\n"
      "v_mfma_f32_16x16x32_bf16 v[28:31], v[120:123], v[136:139], v[28:31]\n"
      "v_add_u32 v195, s62, v195\n"
      "v_mfma_f32_16x16x32_bf16 v[28:31], v[124:127], v[140:143], v[28:31]\n"
      "v_mfma_f32_16x16x32_bf16 v[12:15], v[124:127], v[132:135], v[12:15]\n"
      "v_mfma_f32_16x16x32_bf16 v[12:15], v[120:123], v[128:131], v[12:15]\n"
      "s_waitcnt lgkmcnt(0)\n"
      "v_mfma_f32_16x16x32_bf16 v[0:3], v[218:221], v[64:67], v[0:3]\n"
      "s_barrier\n"
      "v_mfma_f32_16x16x32_bf16 v[0:3], v[222:225], v[68:71], v[0:3]\n"
      "ds_read_b64_tr_b16 v[96:97], v194 offset:0\n"
      "v_mfma_f32_16x16x32_bf16 v[16:19], v[222:225], v[76:79], v[16:19]\n"
      "ds_read_b64_tr_b16 v[98:99], v194 offset:4128\n"
      "v_mfma_f32_16x16x32_bf16 v[16:19], v[218:221], v[72:75], v[16:19]\n"
      "ds_read_b128 v[128:131], v192 offset:16512\n"
      "v_mfma_f32_16x16x32_bf16 v[32:35], v[218:221], v[80:83], v[32:35]\n"
      "ds_read_b64_tr_b16 v[100:101], v194 offset:8256\n"
      "v_mfma_f32_16x16x32_bf16 v[32:35], v[222:225], v[84:87], v[32:35]\n"
      "ds_read_b64_tr_b16 v[102:103], v194 offset:12384\n"
      "v_mfma_f32_16x16x32_bf16 v[48:51], v[222:225], v[92:95], v[48:51]\n"
      "ds_read_b128 v[132:135], v192 offset:24704\n"
      "v_mfma_f32_16x16x32_bf16 v[48:51], v[218:221], v[88:91], v[48:51]\n"
      "ds_read_b128 v[140:143], v192 offset:25728\n"
      "v_mfma_f32_16x16x32_bf16 v[52:55], v[226:229], v[88:91], v[52:55]\n"
      "ds_read_b128 v[136:139], v192 offset:17536\n"
      "v_mfma_f32_16x16x32_bf16 v[52:55], v[230:233], v[92:95], v[52:55]\n"
      "ds_read_b128 v[144:147], v193 offset:16512\n"
      "v_mfma_f32_16x16x32_bf16 v[36:39], v[230:233], v[84:87], v[36:39]\n"
      "ds_read_b128 v[148:151], v193 offset:24704\n"
      "v_mfma_f32_16x16x32_bf16 v[36:39], v[226:229], v[80:83], v[36:39]\n"
      "ds_read_b128 v[156:159], v193 offset:25728\n"
      "v_mfma_f32_16x16x32_bf16 v[20:23], v[226:229], v[72:75], v[20:23]\n"
      "ds_read_b128 v[152:155], v193 offset:17536\n"
      "v_mfma_f32_16x16x32_bf16 v[20:23], v[230:233], v[76:79], v[20:23]\n"
      "ds_read_b64_tr_b16 v[104:105], v194 offset:8\n"
      "v_mfma_f32_16x16x32_bf16 v[4:7], v[230:233], v[68:71], v[4:7]\n"
      "ds_read_b64_tr_b16 v[106:107], v194 offset:4136\n"
      "v_mfma_f32_16x16x32_bf16 v[4:7], v[226:229], v[64:67], v[4:7]\n"
      "ds_read_b64_tr_b16 v[108:109], v194 offset:8264\n"
      "v_mfma_f32_16x16x32_bf16 v[8:11], v[234:237], v[64:67], v[8:11]\n"
      "ds_read_b64_tr_b16 v[110:111], v194 offset:12392\n"
      "v_mfma_f32_16x16x32_bf16 v[8:11], v[238:241], v[68:71], v[8:11]\n"
      "ds_read_b64_tr_b16 v[112:113], v195 offset:0\n"
      "v_mfma_f32_16x16x32_bf16 v[24:27], v[238:241], v[76:79], v[24:27]\n"
      "ds_read_b64_tr_b16 v[114:115], v195 offset:4128\n"
      "v_mfma_f32_16x16x32_bf16 v[24:27], v[234:237], v[72:75], v[24:27]\n"
      "ds_read_b64_tr_b16 v[116:117], v195 offset:8256\n"
      "v_mfma_f32_16x16x32_bf16 v[40:43], v[234:237], v[80:83], v[40:43]\n"
      "ds_read_b64_tr_b16 v[118:119], v195 offset:12384\n"
      "s_add_u32 s61, s61, 1\n"
      "v_mfma_f32_16x16x32_bf16 v[40:43], v[238:241], v[84:87], v[40:43]\n"
      "ds_read_b64_tr_b16 v[120:121], v195 offset:8\n"
      "s_cmp_eq_u32 s61, 3\n"
      "v_mfma_f32_16x16x32_bf16 v[56:59], v[238:241], v[92:95], v[56:59]\n"
      "ds_read_b64_tr_b16 v[122:123], v195 offset:4136\n"
      "s_cselect_b32 s62, s64, s63\n"
      "v_mfma_f32_16x16x32_bf16 v[56:59], v[234:237], v[88:91], v[56:59]\n"
      "ds_read_b64_tr_b16 v[124:125], v195 offset:8264\n"
      "s_cselect_b32 s61, 0, s61\n"
      "v_mfma_f32_16x16x32_bf16 v[60:63], v[242:245], v[88:91], v[60:63]\n"
      "ds_read_b64_tr_b16 v[126:127], v195 offset:12392\n"
      "v_mfma_f32_16x16x32_bf16 v[60:63], v[246:249], v[92:95], v[60:63]\n"
      "v_add_u32 v192, s62, v192\n"
      "v_mfma_f32_16x16x32_bf16 v[44:47], v[246:249], v[84:87], v[44:47]\n"
      "v_add_u32 v193, s62, v193\n"
      "v_mfma_f32_16x16x32_bf16 v[44:47], v[242:245], v[80:83], v[44:47]\n"
      "v_add_u32 v194, s62, v194\n"
      "v_mfma_f32_16x16x32_bf16 v[28:31], v[242:245], v[72:75], v[28:31]\n"
      "v_add_u32 v195, s62, v195\n"
      "v_mfma_f32_16x16x32_bf16 v[28:31], v[246:249], v[76:79], v[28:31]\n"
      "s_sub_u32 s60, s60, 1\n"
      "v_mfma_f32_16x16x32_bf16 v[12:15], v[246:249], v[68:71], v[12:15]\n"
      "s_cmp_lg_u32 s60, 0\n"
      "s_waitcnt vmcnt(0)\n"
      "v_mfma_f32_16x16x32_bf16 v[12:15], v[242:245], v[64:67], v[12:15]\n"
      "s_cbranch_scc1 .Lk_g2\n"
      "s_waitcnt lgkmcnt(0)\n"
      "v_mfma_f32_16x16x32_bf16 v[0:3], v[96:99], v[128:131], v[0:3]\n"
      "s_barrier\n"
      "v_mfma_f32_16x16x32_bf16 v[0:3], v[100:103], v[132:135], v[0:3]\n"
      "ds_read_b64_tr_b16 v[218:219], v194 offset:0\n"
      "v_mfma_f32_16x16x32_bf16 v[16:19], v[100:103], v[140:143], v[16:19]\n"
      "ds_read_b64_tr_b16 v[220:221], v194 offset:4128\n"
      "v_mfma_f32_16x16x32_bf16 v[16:19], v[96:99], v[136:139], v[16:19]\n"
      "ds_read_b128 v[64:67], v192 offset:16512\n"
      "v_mfma_f32_16x16x32_bf16 v[32:35], v[96:99], v[144:147], v[32:35]\n"
      "ds_read_b64_tr_b16 v[222:223], v194 offset:8256\n"
      "v_mfma_f32_16x16x32_bf16 v[32:35], v[100:103], v[148:151], v[32:35]\n"
      "ds_read_b64_tr_b16 v[224:225], v194 offset:12384\n"
      "v_mfma_f32_16x16x32_bf16 v[48:51], v[100:103], v[156:159], v[48:51]\n"
      "ds_read_b128 v[68:71], v192 offset:24704\n"
      "v_mfma_f32_16x16x32_bf16 v[48:51], v[96:99], v[152:155], v[48:51]\n"
      "ds_read_b128 v[76:79], v192 offset:25728\n"
      "v_mfma_f32_16x16x32_bf16 v[52:55], v[104:107], v[152:155], v[52:55]\n"
      "ds_read_b128 v[72:75], v192 offset:17536\n"
      "v_mfma_f32_16x16x32_bf16 v[52:55], v[108:111], v[156:159], v[52:55]\n"
      "ds_read_b128 v[80:83], v193 offset:16512\n"
      "v_mfma_f32_16x16x32_bf16 v[36:39], v[108:111], v[148:151], v[36:39]\n"
      "ds_read_b128 v[84:87], v193 offset:24704\n"
      "v_mfma_f32_16x16x32_bf16 v[36:39], v[104:107], v[144:147], v[36:39]\n"
      "ds_read_b128 v[92:95], v193 offset:25728\n"
      "v_mfma_f32_16x16x32_bf16 v[20:23], v[104:107], v[136:139], v[20:23]\n"
      "ds_read_b128 v[88:91], v193 offset:17536\n"
      "v_mfma_f32_16x16x32_bf16 v[20:23], v[108:111], v[140:143], v[20:23]\n"
      "ds_read_b64_tr_b16 v[226:227], v194 offset:8\n"
      "v_mfma_f32_16x16x32_bf16 v[4:7], v[108:111], v[132:135], v[4:7]\n"
      "ds_read_b64_tr_b16 v[228:229], v194 offset:4136\n"
      "v_mfma_f32_16x16x32_bf16 v[4:7], v[104:107], v[128:131], v[4:7]\n"
      "ds_read_b64_tr_b16 v[230:231], v194 offset:8264\n"
      "v_mfma_f32_16x16x32_bf16 v[8:11], v[112:115], v[128:131], v[8:11]\n"
      "ds_read_b64_tr_b16 v[232:233], v194 offset:12392\n"
      "v_mfma_f32_16x16x32_bf16 v[8:11], v[116:119], v[132:135], v[8:11]\n"
      "ds_read_b64_tr_b16 v[234:235], v195 offset:0\n"
      "v_mfma_f32_16x16x32_bf16 v[24:27], v[116:119], v[140:143], v[24:27]\n"
      "ds_read_b64_tr_b16 v[236:237], v195 offset:4128\n"
      "v_mfma_f32_16x16x32_bf16 v[24:27], v[112:115], v[136:139], v[24:27]\n"
      "ds_read_b64_tr_b16 v[238:239], v195 offset:8256\n"
      "v_mfma_f32_16x16x32_bf16 v[40:43], v[112:115], v[144:147], v[40:43]\n"
      "ds_read_b64_tr_b16 v[240:241], v195 offset:12384\n"
      "s_add_u32 s61, s61, 1\n"
      "v_mfma_f32_16x16x32_bf16 v[40:43], v[116:119], v[148:151], v[40:43]\n"
      "ds_read_b64_tr_b16 v[242:243], v195 offset:8\n"
      "s_cmp_eq_u32 s61, 3\n"
      "v_mfma_f32_16x16x32_bf16 v[56:59], v[116:119], v[156:159], v[56:59]\n"
      "ds_read_b64_tr_b16 v[244:245], v195 offset:4136\n"
      "s_cselect_b32 s62, s64, s63\n"
      "v_mfma_f32_16x16x32_bf16 v[56:59], v[112:115], v[152:155], v[56:59]\n"
      "ds_read_b64_tr_b16 v[246:247], v195 offset:8264\n"
      "s_cselect_b32 s61, 0, s61\n"
      "v_mfma_f32_16x16x32_bf16 v[60:63], v[120:123], v[152:155], v[60:63]\n"
      "ds_read_b64_tr_b16 v[248:249], v195 offset:12392\n"
      "v_mfma_f32_16x16x32_bf16 v[60:63], v[124:127], v[156:159], v[60:63]\n"
      "v_add_u32 v192, s62, v192\n"
      "v_mfma_f32_16x16x32_bf16 v[44:47], v[124:127], v[148:151], v[44:47]\n"
      "v_add_u32 v193, s62, v193\n"
      "v_mfma_f32_16x16x32_bf16 v[44:47], v[120:123], v[144:147], v[44:47]\n"
      "v_add_u32 v194, s62, v194\n"
      "v_mfma_f32_16x16x32_bf16 v[28:31], v[120:123], v[136:139], v[28:31]\n"
      "v_add_u32 v195, s62, v195\n"
      "v_mfma_f32_16x16x32_bf16 v[28:31], v[124:127], v[140:143], v[28:31]\n"
      "v_mfma_f32_16x16x32_bf16 v[12:15], v[124:127], v[132:135], v[12:15]\n"
      "v_mfma_f32_16x16x32_bf16 v[12:15], v[120:123], v[128:131], v[12:15]\n"
      "s_waitcnt lgkmcnt(0)\n"
      "v_mfma_f32_16x16x32_bf16 v[32:35], v[218:221], v[80:83], v[32:35]\n"
      "s_barrier\n"
      "v_mfma_f32_16x16x32_bf16 v[32:35], v[222:225], v[84:87], v[32:35]\n"
      "ds_read_b64_tr_b16 v[96:97], v194 offset:0\n"
      "ds_read_b64_tr_b16 v[98:99], v194 offset:4128\n"
      "v_mfma_f32_16x16x32_bf16 v[48:51], v[222:225], v[92:95], v[48:51]\n"
      "ds_read_b128 v[144:147], v193 offset:16512\n"
      "v_mfma_f32_16x16x32_bf16 v[48:51], v[218:221], v[88:91], v[48:51]\n"
      "ds_read_b64_tr_b16 v[100:101], v194 offset:8256\n"
      "ds_read_b64_tr_b16 v[102:103], v194 offset:12384\n"
      "v_mfma_f32_16x16x32_bf16 v[52:55], v[226:229], v[88:91], v[52:55]\n"
      "ds_read_b128 v[148:151], v193 offset:24704\n"
      "v_mfma_f32_16x16x32_bf16 v[52:55], v[230:233], v[92:95], v[52:55]\n"
      "ds_read_b128 v[156:159], v193 offset:25728\n"
      "ds_read_b128 v[152:155], v193 offset:17536\n"
      "v_mfma_f32_16x16x32_bf16 v[36:39], v[230:233], v[84:87], v[36:39]\n"
      "ds_read_b64_tr_b16 v[104:105], v194 offset:8\n"
      "v_mfma_f32_16x16x32_bf16 v[36:39], v[226:229], v[80:83], v[36:39]\n"
      "ds_read_b64_tr_b16 v[106:107], v194 offset:4136\n"
      "ds_read_b64_tr_b16 v[108:109], v194 offset:8264\n"
      "v_mfma_f32_16x16x32_bf16 v[40:43], v[234:237], v[80:83], v[40:43]\n"
      "ds_read_b64_tr_b16 v[110:111], v194 offset:12392\n"
      "v_mfma_f32_16x16x32_bf16 v[40:43], v[238:241], v[84:87], v[40:43]\n"
      "ds_read_b64_tr_b16 v[112:113], v195 offset:0\n"
      "ds_read_b64_tr_b16 v[114:115], v195 offset:4128\n"
      "v_mfma_f32_16x16x32_bf16 v[56:59], v[238:241], v[92:95], v[56:59]\n"
      "ds_read_b64_tr_b16 v[116:117], v195 offset:8256\n"
      "v_mfma_f32_16x16x32_bf16 v[56:59], v[234:237], v[88:91], v[56:59]\n"
      "ds_read_b64_tr_b16 v[118:119], v195 offset:12384\n"
      "ds_read_b64_tr_b16 v[120:121], v195 offset:8\n"
      "v_mfma_f32_16x16x32_bf16 v[60:63], v[242:245], v[88:91], v[60:63]\n"
      "ds_read_b64_tr_b16 v[122:123], v195 offset:4136\n"
      "v_mfma_f32_16x16x32_bf16 v[60:63], v[246:249], v[92:95], v[60:63]\n"
      "ds_read_b64_tr_b16 v[124:125], v195 offset:8264\n"
      "ds_read_b64_tr_b16 v[126:127], v195 offset:12392\n"
      "v_mfma_f32_16x16x32_bf16 v[44:47], v[246:249], v[84:87], v[44:47]\n"
      "ds_read_b128 v[128:131], v192 offset:16512\n"
      "v_mfma_f32_16x16x32_bf16 v[44:47], v[242:245], v[80:83], v[44:47]\n"
      "ds_read_b128 v[132:135], v192 offset:24704\n"
      "ds_read_b128 v[140:143], v192 offset:25728\n"
      "s_waitcnt lgkmcnt(9)\n"
      "v_mfma_f32_16x16x32_bf16 v[32:35], v[96:99], v[144:147], v[32:35]\n"
      "ds_read_b128 v[136:139], v192 offset:17536\n"
      "v_mfma_f32_16x16x32_bf16 v[32:35], v[100:103], v[148:151], v[32:35]\n"
      "v_mfma_f32_16x16x32_bf16 v[48:51], v[100:103], v[156:159], v[48:51]\n"
      "v_mfma_f32_16x16x32_bf16 v[48:51], v[96:99], v[152:155], v[48:51]\n"
      "v_mfma_f32_16x16x32_bf16 v[52:55], v[104:107], v[152:155], v[52:55]\n"
      "v_mfma_f32_16x16x32_bf16 v[52:55], v[108:111], v[156:159], v[52:55]\n"
      "v_mfma_f32_16x16x32_bf16 v[36:39], v[108:111], v[148:151], v[36:39]\n"
      "v_mfma_f32_16x16x32_bf16 v[36:39], v[104:107], v[144:147], v[36:39]\n"
      "v_mfma_f32_16x16x32_bf16 v[40:43], v[112:115], v[144:147], v[40:43]\n"
      "s_waitcnt lgkmcnt(0)\n"
      "v_mfma_f32_16x16x32_bf16 v[40:43], v[116:119], v[148:151], v[40:43]\n"
      "v_mfma_f32_16x16x32_bf16 v[56:59], v[116:119], v[156:159], v[56:59]\n"
      "v_mfma_f32_16x16x32_bf16 v[56:59], v[112:115], v[152:155], v[56:59]\n"
      "v_mfma_f32_16x16x32_bf16 v[60:63], v[120:123], v[152:155], v[60:63]\n"
      "v_mfma_f32_16x16x32_bf16 v[60:63], v[124:127], v[156:159], v[60:63]\n"
      "v_mfma_f32_16x16x32_bf16 v[44:47], v[124:127], v[148:151], v[44:47]\n"
      "v_mfma_f32_16x16x32_bf16 v[44:47], v[120:123], v[144:147], v[44:47]\n"
      "v_mfma_f32_16x16x32_bf16 v[12:15], v[242:245], v[64:67], v[12:15]\n"
      "v_mfma_f32_16x16x32_bf16 v[12:15], v[246:249], v[68:71], v[12:15]\n"
      "v_mfma_f32_16x16x32_bf16 v[28:31], v[246:249], v[76:79], v[28:31]\n"
      "v_cvt_pk_bf16_f32 v160, v32, v33\n"
      "v_cvt_pk_bf16_f32 v161, v34, v35\n"
      "v_mfma_f32_16x16x32_bf16 v[28:31], v[242:245], v[72:75], v[28:31]\n"
      "v_cvt_pk_bf16_f32 v162, v36, v37\n"
      "v_cvt_pk_bf16_f32 v163, v38, v39\n"
      "v_mfma_f32_16x16x32_bf16 v[24:27], v[234:237], v[72:75], v[24:27]\n"
      "v_min_u32 v160, v160, v199\n"
      "v_min_u32 v161, v161, v199\n"
      "v_mfma_f32_16x16x32_bf16 v[24:27], v[238:241], v[76:79], v[24:27]\n"
      "v_min_u32 v162, v162, v199\n"
      "v_min_u32 v163, v163, v199\n"
      "v_mfma_f32_16x16x32_bf16 v[8:11], v[238:241], v[68:71], v[8:11]\n"
      "buffer_store_dwordx4 v[160:163], v253, s[32:35], s50 offen offset:0 sc1\n"
      "v_cvt_pk_bf16_f32 v168, v48, v49\n"
      "v_mfma_f32_16x16x32_bf16 v[8:11], v[234:237], v[64:67], v[8:11]\n"
      "v_cvt_pk_bf16_f32 v169, v50, v51\n"
      "v_cvt_pk_bf16_f32 v170, v52, v53\n"
      "v_mfma_f32_16x16x32_bf16 v[4:7], v[226:229], v[64:67], v[4:7]\n"
      "v_cvt_pk_bf16_f32 v171, v54, v55\n"
      "v_min_u32 v168, v168, v199\n"
      "v_mfma_f32_16x16x32_bf16 v[4:7], v[230:233], v[68:71], v[4:7]\n"
      "v_min_u32 v169, v169, v199\n"
      "v_min_u32 v170, v170, v199\n"
      "v_mfma_f32_16x16x32_bf16 v[20:23], v[230:233], v[76:79], v[20:23]\n"
      "v_min_u32 v171, v171, v199\n"
      "buffer_store_dwordx4 v[168:171], v253, s[32:35], s50 offen offset:2048 sc1\n"
      "v_mfma_f32_16x16x32_bf16 v[20:23], v[226:229], v[72:75], v[20:23]\n"
      "v_cvt_pk_bf16_f32 v164, v40, v41\n"
      "v_cvt_pk_bf16_f32 v165, v42, v43\n"
      "v_mfma_f32_16x16x32_bf16 v[16:19], v[218:221], v[72:75], v[16:19]\n"
      "v_cvt_pk_bf16_f32 v166, v44, v45\n"
      "v_cvt_pk_bf16_f32 v167, v46, v47\n"
      "v_mfma_f32_16x16x32_bf16 v[16:19], v[222:225], v[76:79], v[16:19]\n"
      "v_min_u32 v164, v164, v199\n"
      "v_min_u32 v165, v165, v199\n"
      "v_mfma_f32_16x16x32_bf16 v[0:3], v[222:225], v[68:71], v[0:3]\n"
      "v_min_u32 v166, v166, v199\n"
      "v_min_u32 v167, v167, v199\n"
      "v_mfma_f32_16x16x32_bf16 v[0:3], v[218:221], v[64:67], v[0:3]\n"
      "buffer_store_dwordx4 v[164:167], v253, s[32:35], s50 offen offset:1024 sc1\n"
      "v_cvt_pk_bf16_f32 v172, v56, v57\n"
      "v_mfma_f32_16x16x32_bf16 v[12:15], v[120:123], v[128:131], v[12:15]\n"
      "v_cvt_pk_bf16_f32 v173, v58, v59\n"
      "v_cvt_pk_bf16_f32 v174, v60, v61\n"
      "v_mfma_f32_16x16x32_bf16 v[12:15], v[124:127], v[132:135], v[12:15]\n"
      "v_cvt_pk_bf16_f32 v175, v62, v63\n"
      "v_min_u32 v172, v172, v199\n"
      "v_mfma_f32_16x16x32_bf16 v[28:31], v[124:127], v[140:143], v[28:31]\n"
      "v_min_u32 v173, v173, v199\n"
      "v_min_u32 v174, v174, v199\n"
      "v_mfma_f32_16x16x32_bf16 v[28:31], v[120:123], v[136:139], v[28:31]\n"
      "v_min_u32 v175, v175, v199\n"
      "buffer_store_dwordx4 v[172:175], v253, s[32:35], s50 offen offset:3072 sc1\n"
      "v_mfma_f32_16x16x32_bf16 v[24:27], v[112:115], v[136:139], v[24:27]\n"
      "v_mfma_f32_16x16x32_bf16 v[24:27], v[116:119], v[140:143], v[24:27]\n"
      "v_mfma_f32_16x16x32_bf16 v[8:11], v[116:119], v[132:135], v[8:11]\n"
      "v_mfma_f32_16x16x32_bf16 v[8:11], v[112:115], v[128:131], v[8:11]\n"
      "v_mfma_f32_16x16x32_bf16 v[4:7], v[104:107], v[128:131], v[4:7]\n"
      "v_mfma_f32_16x16x32_bf16 v[4:7], v[108:111], v[132:135], v[4:7]\n"
      "v_mfma_f32_16x16x32_bf16 v[20:23], v[108:111], v[140:143], v[20:23]\n"
      "v_mfma_f32_16x16x32_bf16 v[20:23], v[104:107], v[136:139], v[20:23]\n"
      "v_mfma_f32_16x16x32_bf16 v[16:19], v[96:99], v[136:139], v[16:19]\n"
      "v_mfma_f32_16x16x32_bf16 v[16:19], v[100:103], v[140:143], v[16:19]\n"
      "v_mfma_f32_16x16x32_bf16 v[0:3], v[100:103], v[132:135], v[0:3]\n"
      "v_mfma_f32_16x16x32_bf16 v[0:3], v[96:99], v[128:131], v[0:3]\n"
      "s_barrier\n"
      "ds_read_b128 v[112:115], v211 offset:0\n"
      "ds_read_b128 v[116:119], v211 offset:256\n"
      "ds_read_b64_tr_b16 v[136:137], v210 offset:0\n"
      "ds_read_b64_tr_b16 v[138:139], v210 offset:512\n"
      "ds_read_b64_tr_b16 v[140:141], v210 offset:4096\n"
      "ds_read_b64_tr_b16 v[142:143], v210 offset:4608\n"
      "ds_read_b64_tr_b16 v[144:145], v210 offset:8\n"
      "ds_read_b64_tr_b16 v[146:147], v210 offset:520\n"
      "ds_read_b64_tr_b16 v[148:149], v210 offset:4104\n"
      "ds_read_b64_tr_b16 v[150:151], v210 offset:4616\n"
      "s_waitcnt lgkmcnt(6)\n"
      "v_lshlrev_b32 v152, 16, v176\n"
      "v_and_b32 v153, v217, v176\n"
      "v_lshlrev_b32 v154, 16, v177\n"
      "v_and_b32 v155, v217, v177\n"
      "v_lshlrev_b32 v156, 16, v178\n"
      "v_and_b32 v157, v217, v178\n"
      "v_lshlrev_b32 v158, 16, v179\n"
      "v_and_b32 v159, v217, v179\n"
      "v_pk_add_f32 v[160:161], v[152:153], v[112:113] op_sel_hi:[1,0]\n"
      "v_pk_add_f32 v[162:163], v[154:155], v[112:113] op_sel_hi:[1,0]\n"
      "v_pk_add_f32 v[164:165], v[156:157], v[112:113] op_sel_hi:[1,0]\n"
      "v_pk_add_f32 v[166:167], v[158:159], v[112:113] op_sel_hi:[1,0]\n"
      "v_pk_add_f32 v[0:1], v[0:1], v[152:153]\n"
      "v_pk_add_f32 v[2:3], v[2:3], v[154:155]\n"
      "v_pk_add_f32 v[4:5], v[4:5], v[156:157]\n"
      "v_pk_add_f32 v[6:7], v[6:7], v[158:159]\n"
      "v_pk_mul_f32 v[160:161], v[160:161], v[114:115] op_sel:[0,1] op_sel_hi:[1,1]\n"
      "v_pk_mul_f32 v[162:163], v[162:163], v[114:115] op_sel:[0,1] op_sel_hi:[1,1]\n"
      "v_pk_mul_f32 v[164:165], v[164:165], v[114:115] op_sel:[0,1] op_sel_hi:[1,1]\n"
      "v_pk_mul_f32 v[166:167], v[166:167], v[114:115] op_sel:[0,1] op_sel_hi:[1,1]\n"
      "v_lshlrev_b32 v152, 16, v136\n"
      "v_and_b32 v153, v217, v136\n"
      "v_lshlrev_b32 v154, 16, v137\n"
      "v_and_b32 v155, v217, v137\n"
      "v_lshlrev_b32 v156, 16, v138\n"
      "v_and_b32 v157, v217, v138\n"
      "v_lshlrev_b32 v158, 16, v139\n"
      "v_and_b32 v159, v217, v139\n"
      "v_pk_fma_f32 v[0:1], v[160:161], v[152:153], v[0:1]\n"
      "v_pk_fma_f32 v[2:3], v[162:163], v[154:155], v[2:3]\n"
      "v_pk_fma_f32 v[4:5], v[164:165], v[156:157], v[4:5]\n"
      "v_pk_fma_f32 v[6:7], v[166:167], v[158:159], v[6:7]\n"
      "s_waitcnt lgkmcnt(4)\n"
      "v_lshlrev_b32 v152, 16, v180\n"
      "v_and_b32 v153, v217, v180\n"
      "v_lshlrev_b32 v154, 16, v181\n"
      "v_and_b32 v155, v217, v181\n"
      "v_lshlrev_b32 v156, 16, v182\n"
      "v_and_b32 v157, v217, v182\n"
      "v_lshlrev_b32 v158, 16, v183\n"
      "v_and_b32 v159, v217, v183\n"
      "v_pk_add_f32 v[160:161], v[152:153], v[112:113] op_sel_hi:[1,0]\n"
      "v_pk_add_f32 v[162:163], v[154:155], v[112:113] op_sel_hi:[1,0]\n"
      "v_pk_add_f32 v[164:165], v[156:157], v[112:113] op_sel_hi:[1,0]\n"
      "v_pk_add_f32 v[166:167], v[158:159], v[112:113] op_sel_hi:[1,0]\n"
      "v_pk_add_f32 v[8:9], v[8:9], v[152:153]\n"
      "v_pk_add_f32 v[10:11], v[10:11], v[154:155]\n"
      "v_pk_add_f32 v[12:13], v[12:13], v[156:157]\n"
      "v_pk_add_f32 v[14:15], v[14:15], v[158:159]\n"
      "v_pk_mul_f32 v[160:161], v[160:161], v[114:115] op_sel:[0,1] op_sel_hi:[1,1]\n"
      "v_pk_mul_f32 v[162:163], v[162:163], v[114:115] op_sel:[0,1] op_sel_hi:[1,1]\n"
      "v_pk_mul_f32 v[164:165], v[164:165], v[114:115] op_sel:[0,1] op_sel_hi:[1,1]\n"
      "v_pk_mul_f32 v[166:167], v[166:167], v[114:115] op_sel:[0,1] op_sel_hi:[1,1]\n"
      "v_lshlrev_b32 v152, 16, v140\n"
      "v_and_b32 v153, v217, v140\n"
      "v_lshlrev_b32 v154, 16, v141\n"
      "v_and_b32 v155, v217, v141\n"
      "v_lshlrev_b32 v156, 16, v142\n"
      "v_and_b32 v157, v217, v142\n"
      "v_lshlrev_b32 v158, 16, v143\n"
      "v_and_b32 v159, v217, v143\n"
      "v_pk_fma_f32 v[8:9], v[160:161], v[152:153], v[8:9]\n"
      "v_pk_fma_f32 v[10:11], v[162:163], v[154:155], v[10:11]\n"
      "v_pk_fma_f32 v[12:13], v[164:165], v[156:157], v[12:13]\n"
      "v_pk_fma_f32 v[14:15], v[166:167], v[158:159], v[14:15]\n"
      "s_waitcnt lgkmcnt(2)\n"
      "v_lshlrev_b32 v152, 16, v184\n"
      "v_and_b32 v153, v217, v184\n"
      "v_lshlrev_b32 v154, 16, v185\n"
      "v_and_b32 v155, v217, v185\n"
      "v_lshlrev_b32 v156, 16, v186\n"
      "v_and_b32 v157, v217, v186\n"
      "v_lshlrev_b32 v158, 16, v187\n"
      "v_and_b32 v159, v217, v187\n"
      "v_pk_add_f32 v[160:161], v[152:153], v[116:117] op_sel_hi:[1,0]\n"
      "v_pk_add_f32 v[162:163], v[154:155], v[116:117] op_sel_hi:[1,0]\n"
      "v_pk_add_f32 v[164:165], v[156:157], v[116:117] op_sel_hi:[1,0]\n"
      "v_pk_add_f32 v[166:167], v[158:159], v[116:117] op_sel_hi:[1,0]\n"
      "v_pk_add_f32 v[16:17], v[16:17], v[152:153]\n"
      "v_pk_add_f32 v[18:19], v[18:19], v[154:155]\n"
      "v_pk_add_f32 v[20:21], v[20:21], v[156:157]\n"
      "v_pk_add_f32 v[22:23], v[22:23], v[158:159]\n"
      "v_pk_mul_f32 v[160:161], v[160:161], v[118:119] op_sel:[0,1] op_sel_hi:[1,1]\n"
      "v_pk_mul_f32 v[162:163], v[162:163], v[118:119] op_sel:[0,1] op_sel_hi:[1,1]\n"
      "v_pk_mul_f32 v[164:165], v[164:165], v[118:119] op_sel:[0,1] op_sel_hi:[1,1]\n"
      "v_pk_mul_f32 v[166:167], v[166:167], v[118:119] op_sel:[0,1] op_sel_hi:[1,1]\n"
      "v_lshlrev_b32 v152, 16, v144\n"
      "v_and_b32 v153, v217, v144\n"
      "v_lshlrev_b32 v154, 16, v145\n"
      "v_and_b32 v155, v217, v145\n"
      "v_lshlrev_b32 v156, 16, v146\n"
      "v_and_b32 v157, v217, v146\n"
      "v_lshlrev_b32 v158, 16, v147\n"
      "v_and_b32 v159, v217, v147\n"
      "v_pk_fma_f32 v[16:17], v[160:161], v[152:153], v[16:17]\n"
      "v_pk_fma_f32 v[18:19], v[162:163], v[154:155], v[18:19]\n"
      "v_pk_fma_f32 v[20:21], v[164:165], v[156:157], v[20:21]\n"
      "v_pk_fma_f32 v[22:23], v[166:167], v[158:159], v[22:23]\n"
      "s_waitcnt lgkmcnt(0)\n"
      "v_lshlrev_b32 v152, 16, v188\n"
      "v_and_b32 v153, v217, v188\n"
      "v_lshlrev_b32 v154, 16, v189\n"
      "v_and_b32 v155, v217, v189\n"
      "v_lshlrev_b32 v156, 16, v190\n"
      "v_and_b32 v157, v217, v190\n"
      "v_lshlrev_b32 v158, 16, v191\n"
      "v_and_b32 v159, v217, v191\n"
      "v_pk_add_f32 v[160:161], v[152:153], v[116:117] op_sel_hi:[1,0]\n"
      "v_pk_add_f32 v[162:163], v[154:155], v[116:117] op_sel_hi:[1,0]\n"
      "v_pk_add_f32 v[164:165], v[156:157], v[116:117] op_sel_hi:[1,0]\n"
      "v_pk_add_f32 v[166:167], v[158:159], v[116:117] op_sel_hi:[1,0]\n"
      "v_pk_add_f32 v[24:25], v[24:25], v[152:153]\n"
      "v_pk_add_f32 v[26:27], v[26:27], v[154:155]\n"
      "v_pk_add_f32 v[28:29], v[28:29], v[156:157]\n"
      "v_pk_add_f32 v[30:31], v[30:31], v[158:159]\n"
      "v_pk_mul_f32 v[160:161], v[160:161], v[118:119] op_sel:[0,1] op_sel_hi:[1,1]\n"
      "v_pk_mul_f32 v[162:163], v[162:163], v[118:119] op_sel:[0,1] op_sel_hi:[1,1]\n"
      "v_pk_mul_f32 v[164:165], v[164:165], v[118:119] op_sel:[0,1] op_sel_hi:[1,1]\n"
      "v_pk_mul_f32 v[166:167], v[166:167], v[118:119] op_sel:[0,1] op_sel_hi:[1,1]\n"
      "v_lshlrev_b32 v152, 16, v148\n"
      "v_and_b32 v153, v217, v148\n"
      "v_lshlrev_b32 v154, 16, v149\n"
      "v_and_b32 v155, v217, v149\n"
      "v_lshlrev_b32 v156, 16, v150\n"
      "v_and_b32 v157, v217, v150\n"
      "v_lshlrev_b32 v158, 16, v151\n"
      "v_and_b32 v159, v217, v151\n"
      "v_pk_fma_f32 v[24:25], v[160:161], v[152:153], v[24:25]\n"
      "v_pk_fma_f32 v[26:27], v[162:163], v[154:155], v[26:27]\n"
      "v_pk_fma_f32 v[28:29], v[164:165], v[156:157], v[28:29]\n"
      "v_pk_fma_f32 v[30:31], v[166:167], v[158:159], v[30:31]\n"
      "s_mov_b32 s56, 60000\n"
      "buffer_load_dwordx4 v[80:83], v253, s[32:35], s51 offen offset:0 sc1\n"
      "buffer_load_dwordx4 v[84:87], v253, s[32:35], s51 offen offset:1024 sc1\n"
      "buffer_load_dwordx4 v[88:91], v253, s[32:35], s51 offen offset:2048 sc1\n"
      "buffer_load_dwordx4 v[92:95], v253, s[32:35], s51 offen offset:3072 sc1\n"
      ".Lk_px:\n"
      "s_waitcnt vmcnt(3)\n"
      "v_lshlrev_b32 v152, 16, v80\n"
      "v_and_b32 v153, v217, v80\n"
      "v_lshlrev_b32 v154, 16, v81\n"
      "v_and_b32 v155, v217, v81\n"
      "v_lshlrev_b32 v156, 16, v82\n"
      "v_and_b32 v157, v217, v82\n"
      "v_lshlrev_b32 v158, 16, v83\n"
      "v_and_b32 v159, v217, v83\n"
      "v_pk_add_f32 v[152:153], v[0:1], v[152:153]\n"
      "v_pk_add_f32 v[154:155], v[2:3], v[154:155]\n"
      "v_pk_add_f32 v[156:157], v[4:5], v[156:157]\n"
      "v_pk_add_f32 v[158:159], v[6:7], v[158:159]\n"
      "v_cvt_pk_bf16_f32 v120, v152, v153\n"
      "v_cvt_pk_bf16_f32 v121, v154, v155\n"
      "v_cvt_pk_bf16_f32 v122, v156, v157\n"
      "v_cvt_pk_bf16_f32 v123, v158, v159\n"
      "ds_write_b128 v208, v[120:123] offset:0\n"
      "v_max3_u32 v79, v80, v81, v82\n"
      "v_max_u32 v79, v79, v83\n"
      "s_waitcnt vmcnt(2)\n"
      "v_lshlrev_b32 v152, 16, v84\n"
      "v_and_b32 v153, v217, v84\n"
      "v_lshlrev_b32 v154, 16, v85\n"
      "v_and_b32 v155, v217, v85\n"
      "v_lshlrev_b32 v156, 16, v86\n"
      "v_and_b32 v157, v217, v86\n"
      "v_lshlrev_b32 v158, 16, v87\n"
      "v_and_b32 v159, v217, v87\n"
      "v_pk_add_f32 v[152:153], v[8:9], v[152:153]\n"
      "v_pk_add_f32 v[154:155], v[10:11], v[154:155]\n"
      "v_pk_add_f32 v[156:157], v[12:13], v[156:157]\n"
      "v_pk_add_f32 v[158:159], v[14:15], v[158:159]\n"
      "v_cvt_pk_bf16_f32 v124, v152, v153\n"
      "v_cvt_pk_bf16_f32 v125, v154, v155\n"
      "v_cvt_pk_bf16_f32 v126, v156, v157\n"
      "v_cvt_pk_bf16_f32 v127, v158, v159\n"
      "ds_write_b128 v209, v[124:127] offset:0\n"
      "ds_read_b128 v[136:139], v214 offset:0\n"
      "ds_read_b128 v[140:143], v215 offset:2048\n"
      "v_max3_u32 v79, v79, v84, v85\n"
      "v_max3_u32 v79, v79, v86, v87\n"
      "s_waitcnt vmcnt(1)\n"
      "v_lshlrev_b32 v152, 16, v88\n"
      "v_and_b32 v153, v217, v88\n"
      "v_lshlrev_b32 v154, 16, v89\n"
      "v_and_b32 v155, v217, v89\n"
      "v_lshlrev_b32 v156, 16, v90\n"
      "v_and_b32 v157, v217, v90\n"
      "v_lshlrev_b32 v158, 16, v91\n"
      "v_and_b32 v159, v217, v91\n"
      "v_pk_add_f32 v[152:153], v[16:17], v[152:153]\n"
      "v_pk_add_f32 v[154:155], v[18:19], v[154:155]\n"
      "v_pk_add_f32 v[156:157], v[20:21], v[156:157]\n"
      "v_pk_add_f32 v[158:159], v[22:23], v[158:159]\n"
      "v_cvt_pk_bf16_f32 v128, v152, v153\n"
      "v_cvt_pk_bf16_f32 v129, v154, v155\n"
      "v_cvt_pk_bf16_f32 v130, v156, v157\n"
      "v_cvt_pk_bf16_f32 v131, v158, v159\n"
      "ds_write_b128 v208, v[128:131] offset:4096\n"
      "v_max3_u32 v79, v79, v88, v89\n"
      "v_max3_u32 v79, v79, v90, v91\n"
      "s_waitcnt vmcnt(0)\n"
      "v_lshlrev_b32 v152, 16, v92\n"
      "v_and_b32 v153, v217, v92\n"
      "v_lshlrev_b32 v154, 16, v93\n"
      "v_and_b32 v155, v217, v93\n"
      "v_lshlrev_b32 v156, 16, v94\n"
      "v_and_b32 v157, v217, v94\n"
      "v_lshlrev_b32 v158, 16, v95\n"
      "v_and_b32 v159, v217, v95\n"
      "v_pk_add_f32 v[152:153], v[24:25], v[152:153]\n"
      "v_pk_add_f32 v[154:155], v[26:27], v[154:155]\n"
      "v_pk_add_f32 v[156:157], v[28:29], v[156:157]\n"
      "v_pk_add_f32 v[158:159], v[30:31], v[158:159]\n"
      "v_cvt_pk_bf16_f32 v132, v152, v153\n"
      "v_cvt_pk_bf16_f32 v133, v154, v155\n"
      "v_cvt_pk_bf16_f32 v134, v156, v157\n"
      "v_cvt_pk_bf16_f32 v135, v158, v159\n"
      "ds_write_b128 v209, v[132:135] offset:4096\n"
      "ds_read_b128 v[144:147], v214 offset:4096\n"
      "ds_read_b128 v[148:151], v215 offset:6144\n"
      "v_max3_u32 v79, v79, v92, v93\n"
      "v_max3_u32 v79, v79, v94, v95\n"
      "v_cmp_eq_u32 vcc, -1, v79\n"
      "s_nop 0\n"
      "s_cbranch_vccz .Lk_pxd\n"
      "buffer_load_dwordx4 v[80:83], v253, s[32:35], s51 offen offset:0 sc1\n"
      "buffer_load_dwordx4 v[84:87], v253, s[32:35], s51 offen offset:1024 sc1\n"
      "buffer_load_dwordx4 v[88:91], v253, s[32:35], s51 offen offset:2048 sc1\n"
      "buffer_load_dwordx4 v[92:95], v253, s[32:35], s51 offen offset:3072 sc1\n"
      "s_sub_u32 s56, s56, 1\n"
      "s_cbranch_scc0 .Lk_px\n"
      ".Lk_pxd:\n"
      "s_waitcnt lgkmcnt(4)\n"
      "buffer_store_dwordx4 v[136:139], v216, s[36:39], s3 offen sc1\n"
      "s_add_u32 s3, s3, 16384\n"
      "s_waitcnt lgkmcnt(4)\n"
      "buffer_store_dwordx4 v[140:143], v216, s[36:39], s3 offen sc1\n"
      "s_add_u32 s3, s3, 16384\n"
      "s_waitcnt lgkmcnt(1)\n"
      "buffer_store_dwordx4 v[144:147], v216, s[36:39], s3 offen sc1\n"
      "s_add_u32 s3, s3, 16384\n"
      "s_waitcnt lgkmcnt(0)\n"
      "buffer_store_dwordx4 v[148:151], v216, s[36:39], s3 offen sc1\n"
      "s_endpgm\n"
      ".Lk_A:\n"
      "s_lshr_b32 s73, s29, 1\n"
      "s_and_b32 s74, s29, 1\n"
      "s_getpc_b64 s[84:85]\n"
      ".Lk_pc9:\n"
      "s_sub_u32 s84, s84, .Lk_pc9-.Lk_start\n"
      "s_subb_u32 s85, s85, 0\n"
      "s_lshl_b32 s52, s31, 2\n"
      "s_add_u32 s52, s52, s29\n"
      "v_lshl_add_u32 v243, v250, 7, s52\n"
      "v_lshlrev_b32 v243, 7, v243\n"
      "v_min_u32 v243, .Lk_end-.Lk_start-4, v243\n"
      "v_lshrrev_b32 v1, 1, v251\n"
      "v_xor_b32 v1, v1, v252\n"
      "v_lshlrev_b32 v1, 4, v1\n"
      "v_lshl_or_b32 v1, v251, 7, v1\n"
      "v_xor_b32 v3, 64, v1\n"
      "s_lshl_b32 s50, s73, 13\n"
      "s_lshl_b32 s51, s74, 12\n"
      "s_add_u32 s50, s50, s51\n"
      "s_xor_b32 s51, s50, 4096\n"
      "s_add_u32 s50, s50, 45056\n"
      "s_add_u32 s51, s51, 45056\n"
      "v_add_u32 v208, s50, v1\n"
      "v_add_u32 v209, s50, v3\n"
      "v_add_u32 v210, s51, v1\n"
      "v_add_u32 v211, s51, v3\n"
      "v_lshrrev_b32 v1, 2, v251\n"
      "v_lshl_add_u32 v1, v252, 3, v1\n"
      "v_mul_u32_u24 v1, 448, v1\n"
      "v_and_b32 v3, 3, v250\n"
      "v_lshl_add_u32 v1, v3, 3, v1\n"
      "v_and_b32 v3, 1, v252\n"
      "v_lshlrev_b32 v3, 5, v3\n"
      "v_xor_b32 v4, 32, v3\n"
      "s_lshl_b32 s50, s74, 6\n"
      "s_add_u32 s50, s50, 45056\n"
      "s_mov_b32 s51, 45440\n"
      "v_add3_u32 v212, v1, v3, s50\n"
      "v_add3_u32 v213, v1, v4, s50\n"
      "v_add3_u32 v214, v1, v3, s51\n"
      "v_add3_u32 v215, v1, v4, s51\n"
      "s_lshl_b32 s50, s29, 9\n"
      "s_xor_b32 s51, s50, 512\n"
      "s_add_u32 s50, s50, 160768\n"
      "s_add_u32 s51, s51, 160768\n"
      "v_lshl_add_u32 v232, v251, 4, s50\n"
      "v_lshl_add_u32 v233, v251, 4, s51\n"
      "s_lshl_b32 s51, s74, 7\n"
      "s_add_u32 s51, s51, 162816\n"
      "v_lshl_add_u32 v234, v252, 4, s51\n"
      "v_lshlrev_b32 v235, 4, v252\n"
      "v_add_u32 v235, 163584, v235\n"
      "s_lshl_b32 s50, s29, 2\n"
      "s_add_u32 s50, s50, 160752\n"
      "v_mov_b32 v240, s50\n"
      "v_mov_b32 v3, 0\n"
      "ds_write_b32 v240, v3\n"
      "v_mov_b32 v4, 160748\n"
      "ds_write_b32 v4, v3\n"
      "v_lshlrev_b32 v4, 2, v251\n"
      "v_add_u32 v4, 160320, v4\n"
      "ds_write_b32 v4, v3\n"
      "s_waitcnt lgkmcnt(0)\n"
      "global_load_dwordx2 v[248:249], v3, s[22:23]\n"
      "s_mov_b32 s32, s22\n"
      "s_and_b32 s33, s23, 0xffff\n"
      "s_mov_b32 s34, 41943040\n"
      "s_mov_b32 s35, 0x20000\n"
      "s_mov_b32 s40, s18\n"
      "s_and_b32 s41, s19, 0xffff\n"
      "s_mov_b32 s42, 7340032\n"
      "s_mov_b32 s43, 0x20000\n"
      "s_mov_b32 s36, s4\n"
      "s_and_b32 s37, s5, 0xffff\n"
      "s_mov_b32 s38, 4194304\n"
      "s_mov_b32 s39, 0x20000\n"
      "v_mov_b32 v236, 0x3f803f80\n"
      "v_mov_b32 v237, 0x3f803f80\n"
      "v_mov_b32 v238, 0x3f803f80\n"
      "v_mov_b32 v239, 0x3f803f80\n"
      "v_cmp_eq_u32 vcc, 0, v251\n"
      "s_nop 1\n"
      "v_cndmask_b32 v216, 0, v236, vcc\n"
      "v_cndmask_b32 v217, 0, v236, vcc\n"
      "v_cndmask_b32 v218, 0, v236, vcc\n"
      "v_cndmask_b32 v219, 0, v236, vcc\n"
      "v_cmp_eq_u32 vcc, 1, v251\n"
      "s_nop 1\n"
      "v_cndmask_b32 v220, 0, v236, vcc\n"
      "v_cndmask_b32 v221, 0, v236, vcc\n"
      "v_cndmask_b32 v222, 0, v236, vcc\n"
      "v_cndmask_b32 v223, 0, v236, vcc\n"
      "v_cmp_eq_u32 vcc, 2, v251\n"
      "s_nop 1\n"
      "v_cndmask_b32 v224, 0, v236, vcc\n"
      "v_cndmask_b32 v225, 0, v236, vcc\n"
      "v_cndmask_b32 v226, 0, v236, vcc\n"
      "v_cndmask_b32 v227, 0, v236, vcc\n"
      "v_cmp_eq_u32 vcc, 3, v251\n"
      "s_nop 1\n"
      "v_cndmask_b32 v228, 0, v236, vcc\n"
      "v_cndmask_b32 v229, 0, v236, vcc\n"
      "v_cndmask_b32 v230, 0, v236, vcc\n"
      "v_cndmask_b32 v231, 0, v236, vcc\n"
      "v_and_b32 v3, 1, v250\n"
      "v_cmp_eq_u32 s[78:79], 1, v3\n"
      "v_and_b32 v3, 2, v250\n"
      "v_cmp_eq_u32 s[80:81], 2, v3\n"
      "v_mov_b32 v4, 0x7fff0000\n"
      "v_lshrrev_b32 v3, 2, v251\n"
      "v_cmp_eq_u32 vcc, v3, v252\n"
      "s_lshl_b32 s50, s29, 8\n"
      "v_lshl_add_u32 v241, v251, 4, s50\n"
      "v_cndmask_b32 v241, v4, v241, vcc\n"
      "s_sub_u32 s50, s74, 2\n"
      "v_lshl_add_u32 v3, v251, 1, s50\n"
      "v_cmp_eq_u32 vcc, 0, v251\n"
      "s_lshl_b32 s50, s73, 6\n"
      "v_lshl_add_u32 v242, v252, 4, s50\n"
      "v_cndmask_b32 v3, v3, 6, vcc\n"
      "v_cmp_gt_u32 vcc, 4, v251\n"
      "v_lshl_add_u32 v242, v3, 7, v242\n"
      "s_lshl_b32 s76, s26, 11\n"
      "v_cndmask_b32 v242, v4, v242, vcc\n"
      "s_add_u32 s76, s76, 1048576\n"
      "s_add_u32 s82, s24, s25\n"
      "s_and_b32 s82, s82, 1\n"
      "s_mov_b32 s60, 6\n"
      "s_mov_b32 s61, 1\n"
      "s_mov_b32 s63, 45056\n"
      "s_mov_b32 s64, 4294877184\n"
      "s_lshr_b32 s50, s24, 3\n"
      "s_mul_i32 s50, s50, 7340032\n"
      "s_and_b32 s51, s24, 7\n"
      "s_lshl_b32 s51, s51, 14\n"
      "s_add_u32 s75, s50, s51\n"
      "s_add_u32 s75, s75, 25165824\n"
      "s_lshl_b32 s51, s29, 11\n"
      "s_add_u32 s50, s75, s51\n"
      "s_mul_i32 s51, s25, 7\n"
      "s_sub_u32 s53, 6, s74\n"
      "s_bitcmp1_b32 s25, 0\n"
      "s_cselect_b32 s52, s53, s74\n"
      "s_cselect_b32 s55, -2, 2\n"
      "s_cselect_b32 s53, 0, 6\n"
      "s_add_u32 s52, s52, s51\n"
      "s_add_u32 s51, s51, s53\n"
      "s_lshr_b32 s53, s51, 1\n"
      "s_lshl_b32 s53, s53, 17\n"
      "s_and_b32 s54, s51, 1\n"
      "s_lshl_b32 s54, s54, 13\n"
      "s_add_u32 s53, s53, s54\n"
      "s_add_u32 s66, s53, s50\n"
      "s_mov_b32 s51, s52\n"
      "s_add_u32 s52, s52, s55\n"
      "s_lshr_b32 s53, s51, 1\n"
      "s_lshl_b32 s53, s53, 17\n"
      "s_and_b32 s54, s51, 1\n"
      "s_lshl_b32 s54, s54, 13\n"
      "s_add_u32 s53, s53, s54\n"
      "s_add_u32 s67, s53, s50\n"
      "s_xor_b32 s70, s67, 2048\n"
      "s_mov_b32 s51, s52\n"
      "s_add_u32 s52, s52, s55\n"
      "s_lshr_b32 s53, s51, 1\n"
      "s_lshl_b32 s53, s53, 17\n"
      "s_and_b32 s54, s51, 1\n"
      "s_lshl_b32 s54, s54, 13\n"
      "s_add_u32 s53, s53, s54\n"
      "s_add_u32 s68, s53, s50\n"
      "s_xor_b32 s71, s68, 2048\n"
      "s_mov_b32 s51, s52\n"
      "s_add_u32 s52, s52, s55\n"
      "s_lshr_b32 s53, s51, 1\n"
      "s_lshl_b32 s53, s53, 17\n"
      "s_and_b32 s54, s51, 1\n"
      "s_lshl_b32 s54, s54, 13\n"
      "s_add_u32 s53, s53, s54\n"
      "s_add_u32 s69, s53, s50\n"
      "s_xor_b32 s72, s69, 2048\n"
      "s_lshl_b32 s55, s26, 14\n"
      "s_lshl_b32 s51, s29, 12\n"
      "s_add_u32 s55, s55, s51\n"
      "s_add_u32 s55, s55, 2097152\n"
      "s_setprio 2\n"
      "s_barrier\n"
      "ds_read_b64_tr_b16 v[176:177], v214 offset:16384\n"
      "ds_read_b64_tr_b16 v[178:179], v214 offset:18176\n"
      "ds_read_b128 v[112:115], v208 offset:0\n"
      "ds_read_b64_tr_b16 v[180:181], v214 offset:30720\n"
      "ds_read_b64_tr_b16 v[182:183], v214 offset:32512\n"
      "ds_read_b128 v[116:119], v209 offset:0\n"
      "ds_read_b128 v[124:127], v209 offset:2048\n"
      "ds_read_b128 v[120:123], v208 offset:2048\n"
      "ds_read_b64_tr_b16 v[184:185], v215 offset:16384\n"
      "ds_read_b64_tr_b16 v[186:187], v215 offset:18176\n"
      "ds_read_b64_tr_b16 v[188:189], v215 offset:30720\n"
      "ds_read_b64_tr_b16 v[190:191], v215 offset:32512\n"
      "ds_read_b64_tr_b16 v[192:193], v212 offset:16384\n"
      "ds_read_b64_tr_b16 v[194:195], v212 offset:18176\n"
      "s_cmp_eq_u32 s82, 0\n"
      "s_cbranch_scc1 .Lk_g1a\n"
      "s_waitcnt lgkmcnt(11)\n"
      "v_mfma_f32_16x16x32_bf16 v[96:99], v[176:179], v[112:115], 0\n"
      "ds_read_b64_tr_b16 v[196:197], v212 offset:30720\n"
      "ds_read_b64_tr_b16 v[200:201], v213 offset:16384\n"
      "s_waitcnt lgkmcnt(10)\n"
      "v_mfma_f32_16x16x32_bf16 v[96:99], v[180:183], v[116:119], v[96:99]\n"
      "ds_read_b64_tr_b16 v[198:199], v212 offset:32512\n"
      "ds_read_b64_tr_b16 v[202:203], v213 offset:18176\n"
      "s_waitcnt lgkmcnt(11)\n"
      "v_mfma_f32_16x16x32_bf16 v[100:103], v[180:183], v[124:127], 0\n"
      "ds_read_b128 v[128:131], v210 offset:0\n"
      "ds_read_b64_tr_b16 v[204:205], v213 offset:30720\n"
      "s_waitcnt lgkmcnt(12)\n"
      "v_mfma_f32_16x16x32_bf16 v[100:103], v[176:179], v[120:123], v[100:103]\n"
      "ds_read_b128 v[132:135], v211 offset:0\n"
      "ds_read_b64_tr_b16 v[206:207], v213 offset:32512\n"
      "s_waitcnt lgkmcnt(12)\n"
      "v_mfma_f32_16x16x32_bf16 v[104:107], v[184:187], v[112:115], 0\n"
      "ds_read_b128 v[140:143], v211 offset:2048\n"
      "ds_read_b64_tr_b16 v[176:177], v212 offset:16512\n"
      "s_waitcnt lgkmcnt(12)\n"
      "v_mfma_f32_16x16x32_bf16 v[104:107], v[188:191], v[116:119], v[104:107]\n"
      "ds_read_b128 v[136:139], v210 offset:2048\n"
      "ds_read_b64_tr_b16 v[178:179], v212 offset:18304\n"
      "v_mfma_f32_16x16x32_bf16 v[108:111], v[188:191], v[124:127], 0\n"
      "ds_read_b64_tr_b16 v[180:181], v212 offset:30848\n"
      "v_mfma_f32_16x16x32_bf16 v[108:111], v[184:187], v[120:123], v[108:111]\n"
      "ds_read_b64_tr_b16 v[182:183], v212 offset:32640\n"
      "s_waitcnt lgkmcnt(14)\n"
      "v_mfma_f32_16x16x32_bf16 v[0:3], v[192:195], v[112:115], 0\n"
      "ds_read_b64_tr_b16 v[184:185], v213 offset:16512\n"
      "s_waitcnt lgkmcnt(12)\n"
      "v_mfma_f32_16x16x32_bf16 v[0:3], v[196:199], v[116:119], v[0:3]\n"
      "ds_read_b64_tr_b16 v[186:187], v213 offset:18304\n"
      "v_mfma_f32_16x16x32_bf16 v[4:7], v[196:199], v[124:127], 0\n"
      "ds_read_b64_tr_b16 v[188:189], v213 offset:30848\n"
      "v_mfma_f32_16x16x32_bf16 v[4:7], v[192:195], v[120:123], v[4:7]\n"
      "ds_read_b64_tr_b16 v[190:191], v213 offset:32640\n"
      "s_waitcnt lgkmcnt(10)\n"
      "v_mfma_f32_16x16x32_bf16 v[8:11], v[192:195], v[128:131], 0\n"
      "s_add_u32 s61, s61, 1\n"
      "v_mfma_f32_16x16x32_bf16 v[8:11], v[196:199], v[132:135], v[8:11]\n"
      "s_cmp_eq_u32 s61, 3\n"
      "s_waitcnt lgkmcnt(6)\n"
      "v_mfma_f32_16x16x32_bf16 v[12:15], v[196:199], v[140:143], 0\n"
      "s_cselect_b32 s62, s64, s63\n"
      "v_mfma_f32_16x16x32_bf16 v[12:15], v[192:195], v[136:139], v[12:15]\n"
      "s_cselect_b32 s61, 0, s61\n"
      "v_mfma_f32_16x16x32_bf16 v[16:19], v[200:203], v[112:115], 0\n"
      "ds_read_b64_tr_b16 v[192:193], v212 offset:16640\n"
      "v_mfma_f32_16x16x32_bf16 v[16:19], v[204:207], v[116:119], v[16:19]\n"
      "ds_read_b64_tr_b16 v[194:195], v212 offset:18432\n"
      "v_mfma_f32_16x16x32_bf16 v[20:23], v[204:207], v[124:127], 0\n"
      "ds_read_b64_tr_b16 v[196:197], v212 offset:30976\n"
      "v_mfma_f32_16x16x32_bf16 v[20:23], v[200:203], v[120:123], v[20:23]\n"
      "ds_read_b64_tr_b16 v[198:199], v212 offset:32768\n"
      "v_mfma_f32_16x16x32_bf16 v[24:27], v[200:203], v[128:131], 0\n"
      "v_add_u32 v208, s62, v208\n"
      "v_mfma_f32_16x16x32_bf16 v[24:27], v[204:207], v[132:135], v[24:27]\n"
      "v_add_u32 v209, s62, v209\n"
      "v_mfma_f32_16x16x32_bf16 v[28:31], v[204:207], v[140:143], 0\n"
      "v_add_u32 v210, s62, v210\n"
      "v_mfma_f32_16x16x32_bf16 v[28:31], v[200:203], v[136:139], v[28:31]\n"
      "v_add_u32 v211, s62, v211\n"
      "v_mfma_f32_16x16x32_bf16 v[32:35], v[176:179], v[112:115], 0\n"
      "ds_read_b64_tr_b16 v[200:201], v213 offset:16640\n"
      "s_waitcnt lgkmcnt(4)\n"
      "v_mfma_f32_16x16x32_bf16 v[32:35], v[180:183], v[116:119], v[32:35]\n"
      "ds_read_b64_tr_b16 v[202:203], v213 offset:18432\n"
      "v_mfma_f32_16x16x32_bf16 v[36:39], v[180:183], v[124:127], 0\n"
      "ds_read_b64_tr_b16 v[204:205], v213 offset:30976\n"
      "v_mfma_f32_16x16x32_bf16 v[36:39], v[176:179], v[120:123], v[36:39]\n"
      "ds_read_b64_tr_b16 v[206:207], v213 offset:32768\n"
      "v_mfma_f32_16x16x32_bf16 v[40:43], v[176:179], v[128:131], 0\n"
      "v_add_u32 v212, s62, v212\n"
      "v_mfma_f32_16x16x32_bf16 v[40:43], v[180:183], v[132:135], v[40:43]\n"
      "v_add_u32 v213, s62, v213\n"
      "v_mfma_f32_16x16x32_bf16 v[44:47], v[180:183], v[140:143], 0\n"
      "v_add_u32 v214, s62, v214\n"
      "v_mfma_f32_16x16x32_bf16 v[44:47], v[176:179], v[136:139], v[44:47]\n"
      "v_add_u32 v215, s62, v215\n"
      "v_mfma_f32_16x16x32_bf16 v[48:51], v[184:187], v[112:115], 0\n"
      "v_mfma_f32_16x16x32_bf16 v[48:51], v[188:191], v[116:119], v[48:51]\n"
      "s_waitcnt lgkmcnt(0)\n"
      "s_barrier\n"
      "v_mfma_f32_16x16x32_bf16 v[52:55], v[188:191], v[124:127], 0\n"
      "ds_read_b64_tr_b16 v[176:177], v214 offset:16384\n"
      "v_mfma_f32_16x16x32_bf16 v[52:55], v[184:187], v[120:123], v[52:55]\n"
      "ds_read_b64_tr_b16 v[178:179], v214 offset:18176\n"
      "v_mfma_f32_16x16x32_bf16 v[56:59], v[184:187], v[128:131], 0\n"
      "ds_read_b64_tr_b16 v[180:181], v214 offset:30720\n"
      "v_mfma_f32_16x16x32_bf16 v[56:59], v[188:191], v[132:135], v[56:59]\n"
      "ds_read_b64_tr_b16 v[182:183], v214 offset:32512\n"
      "v_mfma_f32_16x16x32_bf16 v[60:63], v[188:191], v[140:143], 0\n"
      "ds_read_b128 v[144:147], v208 offset:0\n"
      "v_mfma_f32_16x16x32_bf16 v[60:63], v[184:187], v[136:139], v[60:63]\n"
      "ds_read_b128 v[148:151], v209 offset:0\n"
      "v_mfma_f32_16x16x32_bf16 v[64:67], v[192:195], v[112:115], 0\n"
      "ds_read_b64_tr_b16 v[184:185], v215 offset:16384\n"
      "v_mfma_f32_16x16x32_bf16 v[64:67], v[196:199], v[116:119], v[64:67]\n"
      "ds_read_b64_tr_b16 v[186:187], v215 offset:18176\n"
      "v_mfma_f32_16x16x32_bf16 v[68:71], v[196:199], v[124:127], 0\n"
      "ds_read_b64_tr_b16 v[188:189], v215 offset:30720\n"
      "v_mfma_f32_16x16x32_bf16 v[68:71], v[192:195], v[120:123], v[68:71]\n"
      "ds_read_b64_tr_b16 v[190:191], v215 offset:32512\n"
      "v_mfma_f32_16x16x32_bf16 v[72:75], v[192:195], v[128:131], 0\n"
      "ds_read_b128 v[156:159], v209 offset:2048\n"
      "v_mfma_f32_16x16x32_bf16 v[72:75], v[196:199], v[132:135], v[72:75]\n"
      "ds_read_b128 v[152:155], v208 offset:2048\n"
      "v_mfma_f32_16x16x32_bf16 v[76:79], v[196:199], v[140:143], 0\n"
      "ds_read_b128 v[160:163], v210 offset:0\n"
      "v_mfma_f32_16x16x32_bf16 v[76:79], v[192:195], v[136:139], v[76:79]\n"
      "ds_read_b128 v[164:167], v211 offset:0\n"
      "v_mfma_f32_16x16x32_bf16 v[80:83], v[200:203], v[112:115], 0\n"
      "ds_read_b64_tr_b16 v[192:193], v212 offset:16384\n"
      "v_mfma_f32_16x16x32_bf16 v[80:83], v[204:207], v[116:119], v[80:83]\n"
      "ds_read_b64_tr_b16 v[194:195], v212 offset:18176\n"
      "v_mfma_f32_16x16x32_bf16 v[84:87], v[204:207], v[124:127], 0\n"
      "ds_read_b64_tr_b16 v[196:197], v212 offset:30720\n"
      "v_mfma_f32_16x16x32_bf16 v[84:87], v[200:203], v[120:123], v[84:87]\n"
      "ds_read_b64_tr_b16 v[198:199], v212 offset:32512\n"
      "v_mfma_f32_16x16x32_bf16 v[88:91], v[200:203], v[128:131], 0\n"
      "ds_read_b128 v[172:175], v211 offset:2048\n"
      "v_mfma_f32_16x16x32_bf16 v[88:91], v[204:207], v[132:135], v[88:91]\n"
      "ds_read_b128 v[168:171], v210 offset:2048\n"
      "v_mfma_f32_16x16x32_bf16 v[92:95], v[204:207], v[140:143], 0\n"
      "s_cmp_eq_u32 s73, 0\n"
      "v_mfma_f32_16x16x32_bf16 v[92:95], v[200:203], v[136:139], v[92:95]\n"
      "s_cbranch_scc0 .Lk_g1d\n"
      "s_waitcnt lgkmcnt(6)\n"
      "v_mfma_f32_16x16x32_bf16 v[96:99], v[176:179], v[144:147], v[96:99]\n"
      "ds_read_b64_tr_b16 v[200:201], v213 offset:16384\n"
      "v_mfma_f32_16x16x32_bf16 v[96:99], v[180:183], v[148:151], v[96:99]\n"
      "ds_read_b64_tr_b16 v[202:203], v213 offset:18176\n"
      "v_mfma_f32_16x16x32_bf16 v[112:115], v[144:147], v[144:147], 0\n"
      "v_mfma_f32_16x16x32_bf16 v[112:115], v[148:151], v[148:151], v[112:115]\n"
      "v_mfma_f32_16x16x32_bf16 v[120:123], v[236:239], v[144:147], 0\n"
      "v_mfma_f32_16x16x32_bf16 v[120:123], v[236:239], v[148:151], v[120:123]\n"
      "v_mfma_f32_16x16x32_bf16 v[100:103], v[180:183], v[156:159], v[100:103]\n"
      "v_mfma_f32_16x16x32_bf16 v[244:247], v[176:179], v[216:219], 0\n"
      "v_mfma_f32_16x16x32_bf16 v[244:247], v[180:183], v[216:219], v[244:247]\n"
      "ds_read_b64_tr_b16 v[204:205], v213 offset:30720\n"
      "v_mfma_f32_16x16x32_bf16 v[100:103], v[176:179], v[152:155], v[100:103]\n"
      "ds_read_b64_tr_b16 v[206:207], v213 offset:32512\n"
      "v_mfma_f32_16x16x32_bf16 v[116:119], v[152:155], v[152:155], 0\n"
      "v_mfma_f32_16x16x32_bf16 v[116:119], v[156:159], v[156:159], v[116:119]\n"
      "v_mfma_f32_16x16x32_bf16 v[124:127], v[236:239], v[152:155], 0\n"
      "v_mfma_f32_16x16x32_bf16 v[124:127], v[236:239], v[156:159], v[124:127]\n"
      "v_mfma_f32_16x16x32_bf16 v[104:107], v[184:187], v[144:147], v[104:107]\n"
      "ds_read_b64_tr_b16 v[176:177], v212 offset:16512\n"
      "v_mfma_f32_16x16x32_bf16 v[104:107], v[188:191], v[148:151], v[104:107]\n"
      "ds_read_b64_tr_b16 v[178:179], v212 offset:18304\n"
      "v_mfma_f32_16x16x32_bf16 v[108:111], v[188:191], v[156:159], v[108:111]\n"
      "ds_read_b64_tr_b16 v[180:181], v212 offset:30848\n"
      "s_sleep 3\n"
      "v_mfma_f32_16x16x32_bf16 v[108:111], v[184:187], v[152:155], v[108:111]\n"
      "ds_read_b64_tr_b16 v[182:183], v212 offset:32640\n"
      "s_waitcnt lgkmcnt(4)\n"
      "v_mfma_f32_16x16x32_bf16 v[0:3], v[192:195], v[144:147], v[0:3]\n"
      "ds_read_b64_tr_b16 v[184:185], v213 offset:16512\n"
      "v_mfma_f32_16x16x32_bf16 v[0:3], v[196:199], v[148:151], v[0:3]\n"
      "ds_read_b64_tr_b16 v[186:187], v213 offset:18304\n"
      "v_mfma_f32_16x16x32_bf16 v[4:7], v[196:199], v[156:159], v[4:7]\n"
      "ds_read_b64_tr_b16 v[188:189], v213 offset:30848\n"
      "v_mfma_f32_16x16x32_bf16 v[4:7], v[192:195], v[152:155], v[4:7]\n"
      "ds_read_b64_tr_b16 v[190:191], v213 offset:32640\n"
      "v_mfma_f32_16x16x32_bf16 v[8:11], v[192:195], v[160:163], v[8:11]\n"
      "s_add_u32 s61, s61, 1\n"
      "v_cndmask_b32 v112, v112, v113, s[78:79]\n"
      "v_mfma_f32_16x16x32_bf16 v[8:11], v[196:199], v[164:167], v[8:11]\n"
      "s_cmp_eq_u32 s61, 3\n"
      "v_cndmask_b32 v114, v114, v115, s[78:79]\n"
      "v_mfma_f32_16x16x32_bf16 v[12:15], v[196:199], v[172:175], v[12:15]\n"
      "v_mfma_f32_16x16x32_bf16 v[244:247], v[192:195], v[220:223], v[244:247]\n"
      "v_mfma_f32_16x16x32_bf16 v[244:247], v[196:199], v[220:223], v[244:247]\n"
      "s_cselect_b32 s62, s64, s63\n"
      "v_cndmask_b32 v121, v112, v114, s[80:81]\n"
      "v_mfma_f32_16x16x32_bf16 v[12:15], v[192:195], v[168:171], v[12:15]\n"
      "s_cselect_b32 s61, 0, s61\n"
      "v_cndmask_b32 v116, v116, v117, s[78:79]\n"
      "v_mfma_f32_16x16x32_bf16 v[16:19], v[200:203], v[144:147], v[16:19]\n"
      "ds_read_b64_tr_b16 v[192:193], v212 offset:16640\n"
      "v_cndmask_b32 v118, v118, v119, s[78:79]\n"
      "v_mfma_f32_16x16x32_bf16 v[16:19], v[204:207], v[148:151], v[16:19]\n"
      "ds_read_b64_tr_b16 v[194:195], v212 offset:18432\n"
      "v_cndmask_b32 v123, v116, v118, s[80:81]\n"
      "v_mfma_f32_16x16x32_bf16 v[20:23], v[204:207], v[156:159], v[20:23]\n"
      "ds_read_b64_tr_b16 v[196:197], v212 offset:30976\n"
      "v_mov_b32 v122, v124\n"
      "v_mfma_f32_16x16x32_bf16 v[20:23], v[200:203], v[152:155], v[20:23]\n"
      "ds_read_b64_tr_b16 v[198:199], v212 offset:32768\n"
      "buffer_store_dwordx4 v[120:123], v241, s[32:35], s76 offen offset:1024 sc1\n"
      "v_mfma_f32_16x16x32_bf16 v[24:27], v[200:203], v[160:163], v[24:27]\n"
      "v_add_u32 v208, s62, v208\n"
      "v_mfma_f32_16x16x32_bf16 v[24:27], v[204:207], v[164:167], v[24:27]\n"
      "v_add_u32 v209, s62, v209\n"
      "v_mfma_f32_16x16x32_bf16 v[28:31], v[204:207], v[172:175], v[28:31]\n"
      "v_add_u32 v210, s62, v210\n"
      "v_mfma_f32_16x16x32_bf16 v[28:31], v[200:203], v[168:171], v[28:31]\n"
      "v_add_u32 v211, s62, v211\n"
      "s_waitcnt lgkmcnt(4)\n"
      "v_mfma_f32_16x16x32_bf16 v[32:35], v[176:179], v[144:147], v[32:35]\n"
      "ds_read_b64_tr_b16 v[200:201], v213 offset:16640\n"
      "v_mfma_f32_16x16x32_bf16 v[32:35], v[180:183], v[148:151], v[32:35]\n"
      "ds_read_b64_tr_b16 v[202:203], v213 offset:18432\n"
      "v_mfma_f32_16x16x32_bf16 v[36:39], v[180:183], v[156:159], v[36:39]\n"
      "ds_read_b64_tr_b16 v[204:205], v213 offset:30976\n"
      "v_mfma_f32_16x16x32_bf16 v[36:39], v[176:179], v[152:155], v[36:39]\n"
      "ds_read_b64_tr_b16 v[206:207], v213 offset:32768\n"
      "v_mfma_f32_16x16x32_bf16 v[40:43], v[176:179], v[160:163], v[40:43]\n"
      "v_add_u32 v212, s62, v212\n"
      "v_mfma_f32_16x16x32_bf16 v[40:43], v[180:183], v[164:167], v[40:43]\n"
      "v_add_u32 v213, s62, v213\n"
      "v_mfma_f32_16x16x32_bf16 v[44:47], v[180:183], v[172:175], v[44:47]\n"
      "v_mfma_f32_16x16x32_bf16 v[244:247], v[176:179], v[224:227], v[244:247]\n"
      "v_mfma_f32_16x16x32_bf16 v[244:247], v[180:183], v[224:227], v[244:247]\n"
      "v_add_u32 v214, s62, v214\n"
      "v_mfma_f32_16x16x32_bf16 v[44:47], v[176:179], v[168:171], v[44:47]\n"
      "v_add_u32 v215, s62, v215\n"
      "v_mfma_f32_16x16x32_bf16 v[48:51], v[184:187], v[144:147], v[48:51]\n"
      "v_mfma_f32_16x16x32_bf16 v[48:51], v[188:191], v[148:151], v[48:51]\n"
      "s_waitcnt lgkmcnt(0)\n"
      "s_barrier\n"
      "v_mfma_f32_16x16x32_bf16 v[52:55], v[188:191], v[156:159], v[52:55]\n"
      "ds_read_b64_tr_b16 v[176:177], v214 offset:16384\n"
      "v_mfma_f32_16x16x32_bf16 v[52:55], v[184:187], v[152:155], v[52:55]\n"
      "ds_read_b64_tr_b16 v[178:179], v214 offset:18176\n"
      "v_mfma_f32_16x16x32_bf16 v[56:59], v[184:187], v[160:163], v[56:59]\n"
      "ds_read_b64_tr_b16 v[180:181], v214 offset:30720\n"
      "v_mfma_f32_16x16x32_bf16 v[56:59], v[188:191], v[164:167], v[56:59]\n"
      "ds_read_b64_tr_b16 v[182:183], v214 offset:32512\n"
      "v_mfma_f32_16x16x32_bf16 v[60:63], v[188:191], v[172:175], v[60:63]\n"
      "ds_read_b128 v[112:115], v208 offset:0\n"
      "v_mfma_f32_16x16x32_bf16 v[60:63], v[184:187], v[168:171], v[60:63]\n"
      "ds_read_b128 v[116:119], v209 offset:0\n"
      "v_mfma_f32_16x16x32_bf16 v[64:67], v[192:195], v[144:147], v[64:67]\n"
      "ds_read_b64_tr_b16 v[184:185], v215 offset:16384\n"
      "v_mfma_f32_16x16x32_bf16 v[64:67], v[196:199], v[148:151], v[64:67]\n"
      "ds_read_b64_tr_b16 v[186:187], v215 offset:18176\n"
      "v_mfma_f32_16x16x32_bf16 v[68:71], v[196:199], v[156:159], v[68:71]\n"
      "ds_read_b64_tr_b16 v[188:189], v215 offset:30720\n"
      "v_mfma_f32_16x16x32_bf16 v[68:71], v[192:195], v[152:155], v[68:71]\n"
      "ds_read_b64_tr_b16 v[190:191], v215 offset:32512\n"
      "v_mfma_f32_16x16x32_bf16 v[72:75], v[192:195], v[160:163], v[72:75]\n"
      "ds_read_b128 v[124:127], v209 offset:2048\n"
      "v_mfma_f32_16x16x32_bf16 v[72:75], v[196:199], v[164:167], v[72:75]\n"
      "ds_read_b128 v[120:123], v208 offset:2048\n"
      "v_mfma_f32_16x16x32_bf16 v[76:79], v[196:199], v[172:175], v[76:79]\n"
      "v_mfma_f32_16x16x32_bf16 v[244:247], v[192:195], v[228:231], v[244:247]\n"
      "v_mfma_f32_16x16x32_bf16 v[244:247], v[196:199], v[228:231], v[244:247]\n"
      "ds_read_b128 v[128:131], v210 offset:0\n"
      "v_mfma_f32_16x16x32_bf16 v[76:79], v[192:195], v[168:171], v[76:79]\n"
      "ds_read_b128 v[132:135], v211 offset:0\n"
      "v_mfma_f32_16x16x32_bf16 v[80:83], v[200:203], v[144:147], v[80:83]\n"
      "ds_read_b64_tr_b16 v[192:193], v212 offset:16384\n"
      "v_mfma_f32_16x16x32_bf16 v[80:83], v[204:207], v[148:151], v[80:83]\n"
      "ds_read_b64_tr_b16 v[194:195], v212 offset:18176\n"
      "v_mfma_f32_16x16x32_bf16 v[84:87], v[204:207], v[156:159], v[84:87]\n"
      "ds_read_b64_tr_b16 v[196:197], v212 offset:30720\n"
      "v_mfma_f32_16x16x32_bf16 v[84:87], v[200:203], v[152:155], v[84:87]\n"
      "ds_read_b64_tr_b16 v[198:199], v212 offset:32512\n"
      "v_mfma_f32_16x16x32_bf16 v[88:91], v[200:203], v[160:163], v[88:91]\n"
      "ds_read_b128 v[140:143], v211 offset:2048\n"
      "v_mfma_f32_16x16x32_bf16 v[88:91], v[204:207], v[164:167], v[88:91]\n"
      "ds_read_b128 v[136:139], v210 offset:2048\n"
      "v_mfma_f32_16x16x32_bf16 v[92:95], v[204:207], v[172:175], v[92:95]\n"
      "v_mfma_f32_16x16x32_bf16 v[92:95], v[200:203], v[168:171], v[92:95]\n"
      "buffer_store_dwordx4 v[244:247], v242, s[32:35], s76 offen sc1\n"
      "s_branch .Lk_g1\n"
      ".Lk_g1d:\n"
      "s_waitcnt lgkmcnt(6)\n"
      "v_mfma_f32_16x16x32_bf16 v[96:99], v[176:179], v[144:147], v[96:99]\n"
      "ds_read_b64_tr_b16 v[200:201], v213 offset:16384\n"
      "v_mfma_f32_16x16x32_bf16 v[96:99], v[180:183], v[148:151], v[96:99]\n"
      "ds_read_b64_tr_b16 v[202:203], v213 offset:18176\n"
      "v_mfma_f32_16x16x32_bf16 v[112:115], v[144:147], v[144:147], 0\n"
      "v_mfma_f32_16x16x32_bf16 v[112:115], v[148:151], v[148:151], v[112:115]\n"
      "v_mfma_f32_16x16x32_bf16 v[120:123], v[236:239], v[144:147], 0\n"
      "v_mfma_f32_16x16x32_bf16 v[120:123], v[236:239], v[148:151], v[120:123]\n"
      "v_mfma_f32_16x16x32_bf16 v[100:103], v[180:183], v[156:159], v[100:103]\n"
      "ds_read_b64_tr_b16 v[204:205], v213 offset:30720\n"
      "v_mfma_f32_16x16x32_bf16 v[100:103], v[176:179], v[152:155], v[100:103]\n"
      "ds_read_b64_tr_b16 v[206:207], v213 offset:32512\n"
      "v_mfma_f32_16x16x32_bf16 v[116:119], v[152:155], v[152:155], 0\n"
      "v_mfma_f32_16x16x32_bf16 v[116:119], v[156:159], v[156:159], v[116:119]\n"
      "v_mfma_f32_16x16x32_bf16 v[124:127], v[236:239], v[152:155], 0\n"
      "v_mfma_f32_16x16x32_bf16 v[124:127], v[236:239], v[156:159], v[124:127]\n"
      "v_mfma_f32_16x16x32_bf16 v[104:107], v[184:187], v[144:147], v[104:107]\n"
      "ds_read_b64_tr_b16 v[176:177], v212 offset:16512\n"
      "v_mfma_f32_16x16x32_bf16 v[104:107], v[188:191], v[148:151], v[104:107]\n"
      "ds_read_b64_tr_b16 v[178:179], v212 offset:18304\n"
      "v_mfma_f32_16x16x32_bf16 v[108:111], v[188:191], v[156:159], v[108:111]\n"
      "v_mfma_f32_16x16x32_bf16 v[244:247], v[184:187], v[216:219], 0\n"
      "v_mfma_f32_16x16x32_bf16 v[244:247], v[188:191], v[216:219], v[244:247]\n"
      "ds_read_b64_tr_b16 v[180:181], v212 offset:30848\n"
      "s_sleep 3\n"
      "v_mfma_f32_16x16x32_bf16 v[108:111], v[184:187], v[152:155], v[108:111]\n"
      "ds_read_b64_tr_b16 v[182:183], v212 offset:32640\n"
      "s_waitcnt lgkmcnt(4)\n"
      "v_mfma_f32_16x16x32_bf16 v[0:3], v[192:195], v[144:147], v[0:3]\n"
      "ds_read_b64_tr_b16 v[184:185], v213 offset:16512\n"
      "v_mfma_f32_16x16x32_bf16 v[0:3], v[196:199], v[148:151], v[0:3]\n"
      "ds_read_b64_tr_b16 v[186:187], v213 offset:18304\n"
      "v_mfma_f32_16x16x32_bf16 v[4:7], v[196:199], v[156:159], v[4:7]\n"
      "ds_read_b64_tr_b16 v[188:189], v213 offset:30848\n"
      "v_mfma_f32_16x16x32_bf16 v[4:7], v[192:195], v[152:155], v[4:7]\n"
      "ds_read_b64_tr_b16 v[190:191], v213 offset:32640\n"
      "v_mfma_f32_16x16x32_bf16 v[8:11], v[192:195], v[160:163], v[8:11]\n"
      "s_add_u32 s61, s61, 1\n"
      "v_cndmask_b32 v112, v112, v113, s[78:79]\n"
      "v_mfma_f32_16x16x32_bf16 v[8:11], v[196:199], v[164:167], v[8:11]\n"
      "s_cmp_eq_u32 s61, 3\n"
      "v_cndmask_b32 v114, v114, v115, s[78:79]\n"
      "v_mfma_f32_16x16x32_bf16 v[12:15], v[196:199], v[172:175], v[12:15]\n"
      "s_cselect_b32 s62, s64, s63\n"
      "v_cndmask_b32 v121, v112, v114, s[80:81]\n"
      "v_mfma_f32_16x16x32_bf16 v[12:15], v[192:195], v[168:171], v[12:15]\n"
      "s_cselect_b32 s61, 0, s61\n"
      "v_cndmask_b32 v116, v116, v117, s[78:79]\n"
      "v_mfma_f32_16x16x32_bf16 v[16:19], v[200:203], v[144:147], v[16:19]\n"
      "ds_read_b64_tr_b16 v[192:193], v212 offset:16640\n"
      "v_cndmask_b32 v118, v118, v119, s[78:79]\n"
      "v_mfma_f32_16x16x32_bf16 v[16:19], v[204:207], v[148:151], v[16:19]\n"
      "ds_read_b64_tr_b16 v[194:195], v212 offset:18432\n"
      "v_cndmask_b32 v123, v116, v118, s[80:81]\n"
      "v_mfma_f32_16x16x32_bf16 v[20:23], v[204:207], v[156:159], v[20:23]\n"
      "ds_read_b64_tr_b16 v[196:197], v212 offset:30976\n"
      "v_mov_b32 v122, v124\n"
      "v_mfma_f32_16x16x32_bf16 v[20:23], v[200:203], v[152:155], v[20:23]\n"
      "ds_read_b64_tr_b16 v[198:199], v212 offset:32768\n"
      "buffer_store_dwordx4 v[120:123], v241, s[32:35], s76 offen offset:1024 sc1\n"
      "v_mfma_f32_16x16x32_bf16 v[24:27], v[200:203], v[160:163], v[24:27]\n"
      "v_add_u32 v208, s62, v208\n"
      "v_mfma_f32_16x16x32_bf16 v[24:27], v[204:207], v[164:167], v[24:27]\n"
      "v_add_u32 v209, s62, v209\n"
      "v_mfma_f32_16x16x32_bf16 v[28:31], v[204:207], v[172:175], v[28:31]\n"
      "v_mfma_f32_16x16x32_bf16 v[244:247], v[200:203], v[220:223], v[244:247]\n"
      "v_mfma_f32_16x16x32_bf16 v[244:247], v[204:207], v[220:223], v[244:247]\n"
      "v_add_u32 v210, s62, v210\n"
      "v_mfma_f32_16x16x32_bf16 v[28:31], v[200:203], v[168:171], v[28:31]\n"
      "v_add_u32 v211, s62, v211\n"
      "s_waitcnt lgkmcnt(2)\n"
      "v_mfma_f32_16x16x32_bf16 v[32:35], v[176:179], v[144:147], v[32:35]\n"
      "ds_read_b64_tr_b16 v[200:201], v213 offset:16640\n"
      "v_mfma_f32_16x16x32_bf16 v[32:35], v[180:183], v[148:151], v[32:35]\n"
      "ds_read_b64_tr_b16 v[202:203], v213 offset:18432\n"
      "v_mfma_f32_16x16x32_bf16 v[36:39], v[180:183], v[156:159], v[36:39]\n"
      "ds_read_b64_tr_b16 v[204:205], v213 offset:30976\n"
      "v_mfma_f32_16x16x32_bf16 v[36:39], v[176:179], v[152:155], v[36:39]\n"
      "ds_read_b64_tr_b16 v[206:207], v213 offset:32768\n"
      "v_mfma_f32_16x16x32_bf16 v[40:43], v[176:179], v[160:163], v[40:43]\n"
      "v_add_u32 v212, s62, v212\n"
      "v_mfma_f32_16x16x32_bf16 v[40:43], v[180:183], v[164:167], v[40:43]\n"
      "v_add_u32 v213, s62, v213\n"
      "v_mfma_f32_16x16x32_bf16 v[44:47], v[180:183], v[172:175], v[44:47]\n"
      "v_add_u32 v214, s62, v214\n"
      "v_mfma_f32_16x16x32_bf16 v[44:47], v[176:179], v[168:171], v[44:47]\n"
      "v_add_u32 v215, s62, v215\n"
      "v_mfma_f32_16x16x32_bf16 v[48:51], v[184:187], v[144:147], v[48:51]\n"
      "v_mfma_f32_16x16x32_bf16 v[48:51], v[188:191], v[148:151], v[48:51]\n"
      "s_waitcnt lgkmcnt(0)\n"
      "s_barrier\n"
      "v_mfma_f32_16x16x32_bf16 v[52:55], v[188:191], v[156:159], v[52:55]\n"
      "ds_read_b64_tr_b16 v[176:177], v214 offset:16384\n"
      "v_mfma_f32_16x16x32_bf16 v[52:55], v[184:187], v[152:155], v[52:55]\n"
      "ds_read_b64_tr_b16 v[178:179], v214 offset:18176\n"
      "v_mfma_f32_16x16x32_bf16 v[56:59], v[184:187], v[160:163], v[56:59]\n"
      "ds_read_b64_tr_b16 v[180:181], v214 offset:30720\n"
      "v_mfma_f32_16x16x32_bf16 v[56:59], v[188:191], v[164:167], v[56:59]\n"
      "ds_read_b64_tr_b16 v[182:183], v214 offset:32512\n"
      "v_mfma_f32_16x16x32_bf16 v[60:63], v[188:191], v[172:175], v[60:63]\n"
      "v_mfma_f32_16x16x32_bf16 v[244:247], v[184:187], v[224:227], v[244:247]\n"
      "v_mfma_f32_16x16x32_bf16 v[244:247], v[188:191], v[224:227], v[244:247]\n"
      "ds_read_b128 v[112:115], v208 offset:0\n"
      "v_mfma_f32_16x16x32_bf16 v[60:63], v[184:187], v[168:171], v[60:63]\n"
      "ds_read_b128 v[116:119], v209 offset:0\n"
      "v_mfma_f32_16x16x32_bf16 v[64:67], v[192:195], v[144:147], v[64:67]\n"
      "ds_read_b64_tr_b16 v[184:185], v215 offset:16384\n"
      "v_mfma_f32_16x16x32_bf16 v[64:67], v[196:199], v[148:151], v[64:67]\n"
      "ds_read_b64_tr_b16 v[186:187], v215 offset:18176\n"
      "v_mfma_f32_16x16x32_bf16 v[68:71], v[196:199], v[156:159], v[68:71]\n"
      "ds_read_b64_tr_b16 v[188:189], v215 offset:30720\n"
      "v_mfma_f32_16x16x32_bf16 v[68:71], v[192:195], v[152:155], v[68:71]\n"
      "ds_read_b64_tr_b16 v[190:191], v215 offset:32512\n"
      "v_mfma_f32_16x16x32_bf16 v[72:75], v[192:195], v[160:163], v[72:75]\n"
      "ds_read_b128 v[124:127], v209 offset:2048\n"
      "v_mfma_f32_16x16x32_bf16 v[72:75], v[196:199], v[164:167], v[72:75]\n"
      "ds_read_b128 v[120:123], v208 offset:2048\n"
      "v_mfma_f32_16x16x32_bf16 v[76:79], v[196:199], v[172:175], v[76:79]\n"
      "ds_read_b128 v[128:131], v210 offset:0\n"
      "v_mfma_f32_16x16x32_bf16 v[76:79], v[192:195], v[168:171], v[76:79]\n"
      "ds_read_b128 v[132:135], v211 offset:0\n"
      "v_mfma_f32_16x16x32_bf16 v[80:83], v[200:203], v[144:147], v[80:83]\n"
      "ds_read_b64_tr_b16 v[192:193], v212 offset:16384\n"
      "v_mfma_f32_16x16x32_bf16 v[80:83], v[204:207], v[148:151], v[80:83]\n"
      "ds_read_b64_tr_b16 v[194:195], v212 offset:18176\n"
      "v_mfma_f32_16x16x32_bf16 v[84:87], v[204:207], v[156:159], v[84:87]\n"
      "ds_read_b64_tr_b16 v[196:197], v212 offset:30720\n"
      "v_mfma_f32_16x16x32_bf16 v[84:87], v[200:203], v[152:155], v[84:87]\n"
      "ds_read_b64_tr_b16 v[198:199], v212 offset:32512\n"
      "v_mfma_f32_16x16x32_bf16 v[88:91], v[200:203], v[160:163], v[88:91]\n"
      "ds_read_b128 v[140:143], v211 offset:2048\n"
      "v_mfma_f32_16x16x32_bf16 v[88:91], v[204:207], v[164:167], v[88:91]\n"
      "ds_read_b128 v[136:139], v210 offset:2048\n"
      "v_mfma_f32_16x16x32_bf16 v[92:95], v[204:207], v[172:175], v[92:95]\n"
      "v_mfma_f32_16x16x32_bf16 v[244:247], v[200:203], v[228:231], v[244:247]\n"
      "v_mfma_f32_16x16x32_bf16 v[244:247], v[204:207], v[228:231], v[244:247]\n"
      "v_mfma_f32_16x16x32_bf16 v[92:95], v[200:203], v[168:171], v[92:95]\n"
      "s_nop 6\n"
      "buffer_store_dwordx4 v[244:247], v242, s[32:35], s76 offen sc1\n"
      "s_branch .Lk_g1\n"
      ".Lk_g1a:\n"
      "s_cmp_eq_u32 s73, 0\n"
      "s_cbranch_scc0 .Lk_g1b\n"
      "s_waitcnt lgkmcnt(11)\n"
      "v_mfma_f32_16x16x32_bf16 v[96:99], v[176:179], v[112:115], 0\n"
      "ds_read_b64_tr_b16 v[196:197], v212 offset:30720\n"
      "ds_read_b64_tr_b16 v[200:201], v213 offset:16384\n"
      "s_waitcnt lgkmcnt(10)\n"
      "v_mfma_f32_16x16x32_bf16 v[96:99], v[180:183], v[116:119], v[96:99]\n"
      "ds_read_b64_tr_b16 v[198:199], v212 offset:32512\n"
      "ds_read_b64_tr_b16 v[202:203], v213 offset:18176\n"
      "v_mfma_f32_16x16x32_bf16 v[144:147], v[112:115], v[112:115], 0\n"
      "ds_read_b128 v[128:131], v210 offset:0\n"
      "v_mfma_f32_16x16x32_bf16 v[144:147], v[116:119], v[116:119], v[144:147]\n"
      "ds_read_b128 v[132:135], v211 offset:0\n"
      "v_mfma_f32_16x16x32_bf16 v[152:155], v[236:239], v[112:115], 0\n"
      "ds_read_b128 v[140:143], v211 offset:2048\n"
      "v_mfma_f32_16x16x32_bf16 v[152:155], v[236:239], v[116:119], v[152:155]\n"
      "ds_read_b128 v[136:139], v210 offset:2048\n"
      "s_waitcnt lgkmcnt(14)\n"
      "v_mfma_f32_16x16x32_bf16 v[100:103], v[180:183], v[124:127], 0\n"
      "v_mfma_f32_16x16x32_bf16 v[244:247], v[176:179], v[216:219], 0\n"
      "v_mfma_f32_16x16x32_bf16 v[244:247], v[180:183], v[216:219], v[244:247]\n"
      "ds_read_b64_tr_b16 v[204:205], v213 offset:30720\n"
      "v_mfma_f32_16x16x32_bf16 v[100:103], v[176:179], v[120:123], v[100:103]\n"
      "ds_read_b64_tr_b16 v[206:207], v213 offset:32512\n"
      "v_mfma_f32_16x16x32_bf16 v[148:151], v[120:123], v[120:123], 0\n"
      "v_mfma_f32_16x16x32_bf16 v[148:151], v[124:127], v[124:127], v[148:151]\n"
      "v_mfma_f32_16x16x32_bf16 v[156:159], v[236:239], v[120:123], 0\n"
      "v_mfma_f32_16x16x32_bf16 v[156:159], v[236:239], v[124:127], v[156:159]\n"
      "s_waitcnt lgkmcnt(2)\n"
      "v_mfma_f32_16x16x32_bf16 v[104:107], v[184:187], v[112:115], 0\n"
      "ds_read_b64_tr_b16 v[176:177], v212 offset:16512\n"
      "v_mfma_f32_16x16x32_bf16 v[104:107], v[188:191], v[116:119], v[104:107]\n"
      "ds_read_b64_tr_b16 v[178:179], v212 offset:18304\n"
      "v_mfma_f32_16x16x32_bf16 v[108:111], v[188:191], v[124:127], 0\n"
      "ds_read_b64_tr_b16 v[180:181], v212 offset:30848\n"
      "v_mfma_f32_16x16x32_bf16 v[108:111], v[184:187], v[120:123], v[108:111]\n"
      "ds_read_b64_tr_b16 v[182:183], v212 offset:32640\n"
      "v_mfma_f32_16x16x32_bf16 v[0:3], v[192:195], v[112:115], 0\n"
      "ds_read_b64_tr_b16 v[184:185], v213 offset:16512\n"
      "v_mfma_f32_16x16x32_bf16 v[0:3], v[196:199], v[116:119], v[0:3]\n"
      "ds_read_b64_tr_b16 v[186:187], v213 offset:18304\n"
      "v_mfma_f32_16x16x32_bf16 v[4:7], v[196:199], v[124:127], 0\n"
      "ds_read_b64_tr_b16 v[188:189], v213 offset:30848\n"
      "v_mfma_f32_16x16x32_bf16 v[4:7], v[192:195], v[120:123], v[4:7]\n"
      "ds_read_b64_tr_b16 v[190:191], v213 offset:32640\n"
      "v_mfma_f32_16x16x32_bf16 v[8:11], v[192:195], v[128:131], 0\n"
      "s_add_u32 s61, s61, 1\n"
      "v_cndmask_b32 v144, v144, v145, s[78:79]\n"
      "v_mfma_f32_16x16x32_bf16 v[8:11], v[196:199], v[132:135], v[8:11]\n"
      "s_cmp_eq_u32 s61, 3\n"
      "v_cndmask_b32 v146, v146, v147, s[78:79]\n"
      "v_mfma_f32_16x16x32_bf16 v[12:15], v[196:199], v[140:143], 0\n"
      "v_mfma_f32_16x16x32_bf16 v[244:247], v[192:195], v[220:223], v[244:247]\n"
      "v_mfma_f32_16x16x32_bf16 v[244:247], v[196:199], v[220:223], v[244:247]\n"
      "s_cselect_b32 s62, s64, s63\n"
      "v_cndmask_b32 v153, v144, v146, s[80:81]\n"
      "v_mfma_f32_16x16x32_bf16 v[12:15], v[192:195], v[136:139], v[12:15]\n"
      "s_cselect_b32 s61, 0, s61\n"
      "v_cndmask_b32 v148, v148, v149, s[78:79]\n"
      "v_mfma_f32_16x16x32_bf16 v[16:19], v[200:203], v[112:115], 0\n"
      "ds_read_b64_tr_b16 v[192:193], v212 offset:16640\n"
      "v_cndmask_b32 v150, v150, v151, s[78:79]\n"
      "s_waitcnt lgkmcnt(2)\n"
      "v_mfma_f32_16x16x32_bf16 v[16:19], v[204:207], v[116:119], v[16:19]\n"
      "ds_read_b64_tr_b16 v[194:195], v212 offset:18432\n"
      "v_cndmask_b32 v155, v148, v150, s[80:81]\n"
      "v_mfma_f32_16x16x32_bf16 v[20:23], v[204:207], v[124:127], 0\n"
      "ds_read_b64_tr_b16 v[196:197], v212 offset:30976\n"
      "v_mov_b32 v154, v156\n"
      "v_mfma_f32_16x16x32_bf16 v[20:23], v[200:203], v[120:123], v[20:23]\n"
      "ds_read_b64_tr_b16 v[198:199], v212 offset:32768\n"
      "buffer_store_dwordx4 v[152:155], v241, s[32:35], s76 offen offset:1024 sc1\n"
      "v_mfma_f32_16x16x32_bf16 v[24:27], v[200:203], v[128:131], 0\n"
      "v_add_u32 v208, s62, v208\n"
      "v_mfma_f32_16x16x32_bf16 v[24:27], v[204:207], v[132:135], v[24:27]\n"
      "v_add_u32 v209, s62, v209\n"
      "v_mfma_f32_16x16x32_bf16 v[28:31], v[204:207], v[140:143], 0\n"
      "v_add_u32 v210, s62, v210\n"
      "v_mfma_f32_16x16x32_bf16 v[28:31], v[200:203], v[136:139], v[28:31]\n"
      "v_add_u32 v211, s62, v211\n"
      "v_mfma_f32_16x16x32_bf16 v[32:35], v[176:179], v[112:115], 0\n"
      "ds_read_b64_tr_b16 v[200:201], v213 offset:16640\n"
      "v_mfma_f32_16x16x32_bf16 v[32:35], v[180:183], v[116:119], v[32:35]\n"
      "ds_read_b64_tr_b16 v[202:203], v213 offset:18432\n"
      "v_mfma_f32_16x16x32_bf16 v[36:39], v[180:183], v[124:127], 0\n"
      "ds_read_b64_tr_b16 v[204:205], v213 offset:30976\n"
      "v_mfma_f32_16x16x32_bf16 v[36:39], v[176:179], v[120:123], v[36:39]\n"
      "ds_read_b64_tr_b16 v[206:207], v213 offset:32768\n"
      "v_mfma_f32_16x16x32_bf16 v[40:43], v[176:179], v[128:131], 0\n"
      "v_add_u32 v212, s62, v212\n"
      "v_mfma_f32_16x16x32_bf16 v[40:43], v[180:183], v[132:135], v[40:43]\n"
      "v_add_u32 v213, s62, v213\n"
      "v_mfma_f32_16x16x32_bf16 v[44:47], v[180:183], v[140:143], 0\n"
      "v_mfma_f32_16x16x32_bf16 v[244:247], v[176:179], v[224:227], v[244:247]\n"
      "v_mfma_f32_16x16x32_bf16 v[244:247], v[180:183], v[224:227], v[244:247]\n"
      "v_add_u32 v214, s62, v214\n"
      "v_mfma_f32_16x16x32_bf16 v[44:47], v[176:179], v[136:139], v[44:47]\n"
      "v_add_u32 v215, s62, v215\n"
      "v_mfma_f32_16x16x32_bf16 v[48:51], v[184:187], v[112:115], 0\n"
      "s_waitcnt lgkmcnt(1)\n"
      "v_mfma_f32_16x16x32_bf16 v[48:51], v[188:191], v[116:119], v[48:51]\n"
      "s_waitcnt lgkmcnt(0)\n"
      "s_barrier\n"
      "v_mfma_f32_16x16x32_bf16 v[52:55], v[188:191], v[124:127], 0\n"
      "ds_read_b64_tr_b16 v[176:177], v214 offset:16384\n"
      "v_mfma_f32_16x16x32_bf16 v[52:55], v[184:187], v[120:123], v[52:55]\n"
      "ds_read_b64_tr_b16 v[178:179], v214 offset:18176\n"
      "v_mfma_f32_16x16x32_bf16 v[56:59], v[184:187], v[128:131], 0\n"
      "ds_read_b64_tr_b16 v[180:181], v214 offset:30720\n"
      "v_mfma_f32_16x16x32_bf16 v[56:59], v[188:191], v[132:135], v[56:59]\n"
      "ds_read_b64_tr_b16 v[182:183], v214 offset:32512\n"
      "v_mfma_f32_16x16x32_bf16 v[60:63], v[188:191], v[140:143], 0\n"
      "ds_read_b128 v[144:147], v208 offset:0\n"
      "v_mfma_f32_16x16x32_bf16 v[60:63], v[184:187], v[136:139], v[60:63]\n"
      "ds_read_b128 v[148:151], v209 offset:0\n"
      "v_mfma_f32_16x16x32_bf16 v[64:67], v[192:195], v[112:115], 0\n"
      "ds_read_b64_tr_b16 v[184:185], v215 offset:16384\n"
      "v_mfma_f32_16x16x32_bf16 v[64:67], v[196:199], v[116:119], v[64:67]\n"
      "ds_read_b64_tr_b16 v[186:187], v215 offset:18176\n"
      "v_mfma_f32_16x16x32_bf16 v[68:71], v[196:199], v[124:127], 0\n"
      "ds_read_b64_tr_b16 v[188:189], v215 offset:30720\n"
      "v_mfma_f32_16x16x32_bf16 v[68:71], v[192:195], v[120:123], v[68:71]\n"
      "ds_read_b64_tr_b16 v[190:191], v215 offset:32512\n"
      "v_mfma_f32_16x16x32_bf16 v[72:75], v[192:195], v[128:131], 0\n"
      "ds_read_b128 v[156:159], v209 offset:2048\n"
      "v_mfma_f32_16x16x32_bf16 v[72:75], v[196:199], v[132:135], v[72:75]\n"
      "ds_read_b128 v[152:155], v208 offset:2048\n"
      "v_mfma_f32_16x16x32_bf16 v[76:79], v[196:199], v[140:143], 0\n"
      "v_mfma_f32_16x16x32_bf16 v[244:247], v[192:195], v[228:231], v[244:247]\n"
      "v_mfma_f32_16x16x32_bf16 v[244:247], v[196:199], v[228:231], v[244:247]\n"
      "ds_read_b128 v[160:163], v210 offset:0\n"
      "v_mfma_f32_16x16x32_bf16 v[76:79], v[192:195], v[136:139], v[76:79]\n"
      "ds_read_b128 v[164:167], v211 offset:0\n"
      "v_mfma_f32_16x16x32_bf16 v[80:83], v[200:203], v[112:115], 0\n"
      "ds_read_b64_tr_b16 v[192:193], v212 offset:16384\n"
      "v_mfma_f32_16x16x32_bf16 v[80:83], v[204:207], v[116:119], v[80:83]\n"
      "ds_read_b64_tr_b16 v[194:195], v212 offset:18176\n"
      "v_mfma_f32_16x16x32_bf16 v[84:87], v[204:207], v[124:127], 0\n"
      "ds_read_b64_tr_b16 v[196:197], v212 offset:30720\n"
      "v_mfma_f32_16x16x32_bf16 v[84:87], v[200:203], v[120:123], v[84:87]\n"
      "ds_read_b64_tr_b16 v[198:199], v212 offset:32512\n"
      "v_mfma_f32_16x16x32_bf16 v[88:91], v[200:203], v[128:131], 0\n"
      "ds_read_b128 v[172:175], v211 offset:2048\n"
      "v_mfma_f32_16x16x32_bf16 v[88:91], v[204:207], v[132:135], v[88:91]\n"
      "ds_read_b128 v[168:171], v210 offset:2048\n"
      "v_mfma_f32_16x16x32_bf16 v[92:95], v[204:207], v[140:143], 0\n"
      "v_mfma_f32_16x16x32_bf16 v[92:95], v[200:203], v[136:139], v[92:95]\n"
      "s_branch .Lk_g1c\n"
      ".Lk_g1b:\n"
      "s_waitcnt lgkmcnt(11)\n"
      "v_mfma_f32_16x16x32_bf16 v[96:99], v[176:179], v[112:115], 0\n"
      "ds_read_b64_tr_b16 v[196:197], v212 offset:30720\n"
      "ds_read_b64_tr_b16 v[200:201], v213 offset:16384\n"
      "s_waitcnt lgkmcnt(10)\n"
      "v_mfma_f32_16x16x32_bf16 v[96:99], v[180:183], v[116:119], v[96:99]\n"
      "ds_read_b64_tr_b16 v[198:199], v212 offset:32512\n"
      "ds_read_b64_tr_b16 v[202:203], v213 offset:18176\n"
      "v_mfma_f32_16x16x32_bf16 v[144:147], v[112:115], v[112:115], 0\n"
      "ds_read_b128 v[128:131], v210 offset:0\n"
      "v_mfma_f32_16x16x32_bf16 v[144:147], v[116:119], v[116:119], v[144:147]\n"
      "ds_read_b128 v[132:135], v211 offset:0\n"
      "v_mfma_f32_16x16x32_bf16 v[152:155], v[236:239], v[112:115], 0\n"
      "ds_read_b128 v[140:143], v211 offset:2048\n"
      "v_mfma_f32_16x16x32_bf16 v[152:155], v[236:239], v[116:119], v[152:155]\n"
      "ds_read_b128 v[136:139], v210 offset:2048\n"
      "s_waitcnt lgkmcnt(14)\n"
      "v_mfma_f32_16x16x32_bf16 v[100:103], v[180:183], v[124:127], 0\n"
      "ds_read_b64_tr_b16 v[204:205], v213 offset:30720\n"
      "v_mfma_f32_16x16x32_bf16 v[100:103], v[176:179], v[120:123], v[100:103]\n"
      "ds_read_b64_tr_b16 v[206:207], v213 offset:32512\n"
      "v_mfma_f32_16x16x32_bf16 v[148:151], v[120:123], v[120:123], 0\n"
      "v_mfma_f32_16x16x32_bf16 v[148:151], v[124:127], v[124:127], v[148:151]\n"
      "v_mfma_f32_16x16x32_bf16 v[156:159], v[236:239], v[120:123], 0\n"
      "v_mfma_f32_16x16x32_bf16 v[156:159], v[236:239], v[124:127], v[156:159]\n"
      "s_waitcnt lgkmcnt(4)\n"
      "v_mfma_f32_16x16x32_bf16 v[104:107], v[184:187], v[112:115], 0\n"
      "ds_read_b64_tr_b16 v[176:177], v212 offset:16512\n"
      "v_mfma_f32_16x16x32_bf16 v[104:107], v[188:191], v[116:119], v[104:107]\n"
      "ds_read_b64_tr_b16 v[178:179], v212 offset:18304\n"
      "v_mfma_f32_16x16x32_bf16 v[108:111], v[188:191], v[124:127], 0\n"
      "v_mfma_f32_16x16x32_bf16 v[244:247], v[184:187], v[216:219], 0\n"
      "v_mfma_f32_16x16x32_bf16 v[244:247], v[188:191], v[216:219], v[244:247]\n"
      "ds_read_b64_tr_b16 v[180:181], v212 offset:30848\n"
      "v_mfma_f32_16x16x32_bf16 v[108:111], v[184:187], v[120:123], v[108:111]\n"
      "ds_read_b64_tr_b16 v[182:183], v212 offset:32640\n"
      "v_mfma_f32_16x16x32_bf16 v[0:3], v[192:195], v[112:115], 0\n"
      "ds_read_b64_tr_b16 v[184:185], v213 offset:16512\n"
      "v_mfma_f32_16x16x32_bf16 v[0:3], v[196:199], v[116:119], v[0:3]\n"
      "ds_read_b64_tr_b16 v[186:187], v213 offset:18304\n"
      "v_mfma_f32_16x16x32_bf16 v[4:7], v[196:199], v[124:127], 0\n"
      "ds_read_b64_tr_b16 v[188:189], v213 offset:30848\n"
      "v_mfma_f32_16x16x32_bf16 v[4:7], v[192:195], v[120:123], v[4:7]\n"
      "ds_read_b64_tr_b16 v[190:191], v213 offset:32640\n"
      "v_mfma_f32_16x16x32_bf16 v[8:11], v[192:195], v[128:131], 0\n"
      "s_add_u32 s61, s61, 1\n"
      "v_cndmask_b32 v144, v144, v145, s[78:79]\n"
      "v_mfma_f32_16x16x32_bf16 v[8:11], v[196:199], v[132:135], v[8:11]\n"
      "s_cmp_eq_u32 s61, 3\n"
      "v_cndmask_b32 v146, v146, v147, s[78:79]\n"
      "s_waitcnt lgkmcnt(6)\n"
      "v_mfma_f32_16x16x32_bf16 v[12:15], v[196:199], v[140:143], 0\n"
      "s_cselect_b32 s62, s64, s63\n"
      "v_cndmask_b32 v153, v144, v146, s[80:81]\n"
      "v_mfma_f32_16x16x32_bf16 v[12:15], v[192:195], v[136:139], v[12:15]\n"
      "s_cselect_b32 s61, 0, s61\n"
      "v_cndmask_b32 v148, v148, v149, s[78:79]\n"
      "v_mfma_f32_16x16x32_bf16 v[16:19], v[200:203], v[112:115], 0\n"
      "ds_read_b64_tr_b16 v[192:193], v212 offset:16640\n"
      "v_cndmask_b32 v150, v150, v151, s[78:79]\n"
      "v_mfma_f32_16x16x32_bf16 v[16:19], v[204:207], v[116:119], v[16:19]\n"
      "ds_read_b64_tr_b16 v[194:195], v212 offset:18432\n"
      "v_cndmask_b32 v155, v148, v150, s[80:81]\n"
      "v_mfma_f32_16x16x32_bf16 v[20:23], v[204:207], v[124:127], 0\n"
      "ds_read_b64_tr_b16 v[196:197], v212 offset:30976\n"
      "v_mov_b32 v154, v156\n"
      "v_mfma_f32_16x16x32_bf16 v[20:23], v[200:203], v[120:123], v[20:23]\n"
      "ds_read_b64_tr_b16 v[198:199], v212 offset:32768\n"
      "buffer_store_dwordx4 v[152:155], v241, s[32:35], s76 offen offset:1024 sc1\n"
      "v_mfma_f32_16x16x32_bf16 v[24:27], v[200:203], v[128:131], 0\n"
      "v_add_u32 v208, s62, v208\n"
      "v_mfma_f32_16x16x32_bf16 v[24:27], v[204:207], v[132:135], v[24:27]\n"
      "v_add_u32 v209, s62, v209\n"
      "v_mfma_f32_16x16x32_bf16 v[28:31], v[204:207], v[140:143], 0\n"
      "v_mfma_f32_16x16x32_bf16 v[244:247], v[200:203], v[220:223], v[244:247]\n"
      "v_mfma_f32_16x16x32_bf16 v[244:247], v[204:207], v[220:223], v[244:247]\n"
      "v_add_u32 v210, s62, v210\n"
      "v_mfma_f32_16x16x32_bf16 v[28:31], v[200:203], v[136:139], v[28:31]\n"
      "v_add_u32 v211, s62, v211\n"
      "v_mfma_f32_16x16x32_bf16 v[32:35], v[176:179], v[112:115], 0\n"
      "ds_read_b64_tr_b16 v[200:201], v213 offset:16640\n"
      "s_waitcnt lgkmcnt(2)\n"
      "v_mfma_f32_16x16x32_bf16 v[32:35], v[180:183], v[116:119], v[32:35]\n"
      "ds_read_b64_tr_b16 v[202:203], v213 offset:18432\n"
      "v_mfma_f32_16x16x32_bf16 v[36:39], v[180:183], v[124:127], 0\n"
      "ds_read_b64_tr_b16 v[204:205], v213 offset:30976\n"
      "v_mfma_f32_16x16x32_bf16 v[36:39], v[176:179], v[120:123], v[36:39]\n"
      "ds_read_b64_tr_b16 v[206:207], v213 offset:32768\n"
      "v_mfma_f32_16x16x32_bf16 v[40:43], v[176:179], v[128:131], 0\n"
      "v_add_u32 v212, s62, v212\n"
      "v_mfma_f32_16x16x32_bf16 v[40:43], v[180:183], v[132:135], v[40:43]\n"
      "v_add_u32 v213, s62, v213\n"
      "v_mfma_f32_16x16x32_bf16 v[44:47], v[180:183], v[140:143], 0\n"
      "v_add_u32 v214, s62, v214\n"
      "v_mfma_f32_16x16x32_bf16 v[44:47], v[176:179], v[136:139], v[44:47]\n"
      "v_add_u32 v215, s62, v215\n"
      "v_mfma_f32_16x16x32_bf16 v[48:51], v[184:187], v[112:115], 0\n"
      "v_mfma_f32_16x16x32_bf16 v[48:51], v[188:191], v[116:119], v[48:51]\n"
      "s_waitcnt lgkmcnt(0)\n"
      "s_barrier\n"
      "v_mfma_f32_16x16x32_bf16 v[52:55], v[188:191], v[124:127], 0\n"
      "ds_read_b64_tr_b16 v[176:177], v214 offset:16384\n"
      "v_mfma_f32_16x16x32_bf16 v[52:55], v[184:187], v[120:123], v[52:55]\n"
      "ds_read_b64_tr_b16 v[178:179], v214 offset:18176\n"
      "v_mfma_f32_16x16x32_bf16 v[56:59], v[184:187], v[128:131], 0\n"
      "ds_read_b64_tr_b16 v[180:181], v214 offset:30720\n"
      "v_mfma_f32_16x16x32_bf16 v[56:59], v[188:191], v[132:135], v[56:59]\n"
      "ds_read_b64_tr_b16 v[182:183], v214 offset:32512\n"
      "v_mfma_f32_16x16x32_bf16 v[60:63], v[188:191], v[140:143], 0\n"
      "v_mfma_f32_16x16x32_bf16 v[244:247], v[184:187], v[224:227], v[244:247]\n"
      "v_mfma_f32_16x16x32_bf16 v[244:247], v[188:191], v[224:227], v[244:247]\n"
      "ds_read_b128 v[144:147], v208 offset:0\n"
      "v_mfma_f32_16x16x32_bf16 v[60:63], v[184:187], v[136:139], v[60:63]\n"
      "ds_read_b128 v[148:151], v209 offset:0\n"
      "v_mfma_f32_16x16x32_bf16 v[64:67], v[192:195], v[112:115], 0\n"
      "ds_read_b64_tr_b16 v[184:185], v215 offset:16384\n"
      "v_mfma_f32_16x16x32_bf16 v[64:67], v[196:199], v[116:119], v[64:67]\n"
      "ds_read_b64_tr_b16 v[186:187], v215 offset:18176\n"
      "v_mfma_f32_16x16x32_bf16 v[68:71], v[196:199], v[124:127], 0\n"
      "ds_read_b64_tr_b16 v[188:189], v215 offset:30720\n"
      "v_mfma_f32_16x16x32_bf16 v[68:71], v[192:195], v[120:123], v[68:71]\n"
      "ds_read_b64_tr_b16 v[190:191], v215 offset:32512\n"
      "v_mfma_f32_16x16x32_bf16 v[72:75], v[192:195], v[128:131], 0\n"
      "ds_read_b128 v[156:159], v209 offset:2048\n"
      "v_mfma_f32_16x16x32_bf16 v[72:75], v[196:199], v[132:135], v[72:75]\n"
      "ds_read_b128 v[152:155], v208 offset:2048\n"
      "v_mfma_f32_16x16x32_bf16 v[76:79], v[196:199], v[140:143], 0\n"
      "ds_read_b128 v[160:163], v210 offset:0\n"
      "v_mfma_f32_16x16x32_bf16 v[76:79], v[192:195], v[136:139], v[76:79]\n"
      "ds_read_b128 v[164:167], v211 offset:0\n"
      "v_mfma_f32_16x16x32_bf16 v[80:83], v[200:203], v[112:115], 0\n"
      "ds_read_b64_tr_b16 v[192:193], v212 offset:16384\n"
      "v_mfma_f32_16x16x32_bf16 v[80:83], v[204:207], v[116:119], v[80:83]\n"
      "ds_read_b64_tr_b16 v[194:195], v212 offset:18176\n"
      "v_mfma_f32_16x16x32_bf16 v[84:87], v[204:207], v[124:127], 0\n"
      "ds_read_b64_tr_b16 v[196:197], v212 offset:30720\n"
      "v_mfma_f32_16x16x32_bf16 v[84:87], v[200:203], v[120:123], v[84:87]\n"
      "ds_read_b64_tr_b16 v[198:199], v212 offset:32512\n"
      "v_mfma_f32_16x16x32_bf16 v[88:91], v[200:203], v[128:131], 0\n"
      "ds_read_b128 v[172:175], v211 offset:2048\n"
      "v_mfma_f32_16x16x32_bf16 v[88:91], v[204:207], v[132:135], v[88:91]\n"
      "ds_read_b128 v[168:171], v210 offset:2048\n"
      "v_mfma_f32_16x16x32_bf16 v[92:95], v[204:207], v[140:143], 0\n"
      "v_mfma_f32_16x16x32_bf16 v[244:247], v[200:203], v[228:231], v[244:247]\n"
      "v_mfma_f32_16x16x32_bf16 v[244:247], v[204:207], v[228:231], v[244:247]\n"
      "v_mfma_f32_16x16x32_bf16 v[92:95], v[200:203], v[136:139], v[92:95]\n"
      ".Lk_g1c:\n"
      "s_waitcnt lgkmcnt(6)\n"
      "v_mfma_f32_16x16x32_bf16 v[96:99], v[176:179], v[144:147], v[96:99]\n"
      "ds_read_b64_tr_b16 v[200:201], v213 offset:16384\n"
      "v_mfma_f32_16x16x32_bf16 v[96:99], v[180:183], v[148:151], v[96:99]\n"
      "ds_read_b64_tr_b16 v[202:203], v213 offset:18176\n"
      "v_mfma_f32_16x16x32_bf16 v[100:103], v[180:183], v[156:159], v[100:103]\n"
      "ds_read_b64_tr_b16 v[204:205], v213 offset:30720\n"
      "v_mfma_f32_16x16x32_bf16 v[100:103], v[176:179], v[152:155], v[100:103]\n"
      "ds_read_b64_tr_b16 v[206:207], v213 offset:32512\n"
      "v_mfma_f32_16x16x32_bf16 v[104:107], v[184:187], v[144:147], v[104:107]\n"
      "ds_read_b64_tr_b16 v[176:177], v212 offset:16512\n"
      "v_mfma_f32_16x16x32_bf16 v[104:107], v[188:191], v[148:151], v[104:107]\n"
      "ds_read_b64_tr_b16 v[178:179], v212 offset:18304\n"
      "v_mfma_f32_16x16x32_bf16 v[108:111], v[188:191], v[156:159], v[108:111]\n"
      "ds_read_b64_tr_b16 v[180:181], v212 offset:30848\n"
      "s_sleep 3\n"
      "v_mfma_f32_16x16x32_bf16 v[108:111], v[184:187], v[152:155], v[108:111]\n"
      "ds_read_b64_tr_b16 v[182:183], v212 offset:32640\n"
      "s_waitcnt lgkmcnt(8)\n"
      "v_mfma_f32_16x16x32_bf16 v[0:3], v[192:195], v[144:147], v[0:3]\n"
      "ds_read_b64_tr_b16 v[184:185], v213 offset:16512\n"
      "v_mfma_f32_16x16x32_bf16 v[0:3], v[196:199], v[148:151], v[0:3]\n"
      "ds_read_b64_tr_b16 v[186:187], v213 offset:18304\n"
      "v_mfma_f32_16x16x32_bf16 v[4:7], v[196:199], v[156:159], v[4:7]\n"
      "ds_read_b64_tr_b16 v[188:189], v213 offset:30848\n"
      "v_mfma_f32_16x16x32_bf16 v[4:7], v[192:195], v[152:155], v[4:7]\n"
      "ds_read_b64_tr_b16 v[190:191], v213 offset:32640\n"
      "v_mfma_f32_16x16x32_bf16 v[8:11], v[192:195], v[160:163], v[8:11]\n"
      "s_add_u32 s61, s61, 1\n"
      "buffer_store_dwordx4 v[244:247], v242, s[32:35], s76 offen sc1\n"
      "v_mfma_f32_16x16x32_bf16 v[8:11], v[196:199], v[164:167], v[8:11]\n"
      "s_cmp_eq_u32 s61, 3\n"
      "v_mfma_f32_16x16x32_bf16 v[12:15], v[196:199], v[172:175], v[12:15]\n"
      "s_cselect_b32 s62, s64, s63\n"
      "v_mfma_f32_16x16x32_bf16 v[12:15], v[192:195], v[168:171], v[12:15]\n"
      "s_cselect_b32 s61, 0, s61\n"
      "s_waitcnt lgkmcnt(4)\n"
      "v_mfma_f32_16x16x32_bf16 v[16:19], v[200:203], v[144:147], v[16:19]\n"
      "ds_read_b64_tr_b16 v[192:193], v212 offset:16640\n"
      "v_mfma_f32_16x16x32_bf16 v[16:19], v[204:207], v[148:151], v[16:19]\n"
      "ds_read_b64_tr_b16 v[194:195], v212 offset:18432\n"
      "v_mfma_f32_16x16x32_bf16 v[20:23], v[204:207], v[156:159], v[20:23]\n"
      "ds_read_b64_tr_b16 v[196:197], v212 offset:30976\n"
      "v_mfma_f32_16x16x32_bf16 v[20:23], v[200:203], v[152:155], v[20:23]\n"
      "ds_read_b64_tr_b16 v[198:199], v212 offset:32768\n"
      "v_mfma_f32_16x16x32_bf16 v[24:27], v[200:203], v[160:163], v[24:27]\n"
      "v_add_u32 v208, s62, v208\n"
      "v_mfma_f32_16x16x32_bf16 v[24:27], v[204:207], v[164:167], v[24:27]\n"
      "v_add_u32 v209, s62, v209\n"
      "v_mfma_f32_16x16x32_bf16 v[28:31], v[204:207], v[172:175], v[28:31]\n"
      "v_add_u32 v210, s62, v210\n"
      "v_mfma_f32_16x16x32_bf16 v[28:31], v[200:203], v[168:171], v[28:31]\n"
      "v_add_u32 v211, s62, v211\n"
      "v_mfma_f32_16x16x32_bf16 v[32:35], v[176:179], v[144:147], v[32:35]\n"
      "ds_read_b64_tr_b16 v[200:201], v213 offset:16640\n"
      "v_mfma_f32_16x16x32_bf16 v[32:35], v[180:183], v[148:151], v[32:35]\n"
      "ds_read_b64_tr_b16 v[202:203], v213 offset:18432\n"
      "v_mfma_f32_16x16x32_bf16 v[36:39], v[180:183], v[156:159], v[36:39]\n"
      "ds_read_b64_tr_b16 v[204:205], v213 offset:30976\n"
      "v_mfma_f32_16x16x32_bf16 v[36:39], v[176:179], v[152:155], v[36:39]\n"
      "ds_read_b64_tr_b16 v[206:207], v213 offset:32768\n"
      "v_mfma_f32_16x16x32_bf16 v[40:43], v[176:179], v[160:163], v[40:43]\n"
      "v_add_u32 v212, s62, v212\n"
      "v_mfma_f32_16x16x32_bf16 v[40:43], v[180:183], v[164:167], v[40:43]\n"
      "v_add_u32 v213, s62, v213\n"
      "v_mfma_f32_16x16x32_bf16 v[44:47], v[180:183], v[172:175], v[44:47]\n"
      "v_add_u32 v214, s62, v214\n"
      "v_mfma_f32_16x16x32_bf16 v[44:47], v[176:179], v[168:171], v[44:47]\n"
      "v_add_u32 v215, s62, v215\n"
      "s_waitcnt lgkmcnt(4)\n"
      "v_mfma_f32_16x16x32_bf16 v[48:51], v[184:187], v[144:147], v[48:51]\n"
      "v_mfma_f32_16x16x32_bf16 v[48:51], v[188:191], v[148:151], v[48:51]\n"
      "s_waitcnt lgkmcnt(0)\n"
      "s_barrier\n"
      "v_mfma_f32_16x16x32_bf16 v[52:55], v[188:191], v[156:159], v[52:55]\n"
      "ds_read_b64_tr_b16 v[176:177], v214 offset:16384\n"
      "v_mfma_f32_16x16x32_bf16 v[52:55], v[184:187], v[152:155], v[52:55]\n"
      "ds_read_b64_tr_b16 v[178:179], v214 offset:18176\n"
      "v_mfma_f32_16x16x32_bf16 v[56:59], v[184:187], v[160:163], v[56:59]\n"
      "ds_read_b64_tr_b16 v[180:181], v214 offset:30720\n"
      "v_mfma_f32_16x16x32_bf16 v[56:59], v[188:191], v[164:167], v[56:59]\n"
      "ds_read_b64_tr_b16 v[182:183], v214 offset:32512\n"
      "v_mfma_f32_16x16x32_bf16 v[60:63], v[188:191], v[172:175], v[60:63]\n"
      "ds_read_b128 v[112:115], v208 offset:0\n"
      "v_mfma_f32_16x16x32_bf16 v[60:63], v[184:187], v[168:171], v[60:63]\n"
      "ds_read_b128 v[116:119], v209 offset:0\n"
      "v_mfma_f32_16x16x32_bf16 v[64:67], v[192:195], v[144:147], v[64:67]\n"
      "ds_read_b64_tr_b16 v[184:185], v215 offset:16384\n"
      "v_mfma_f32_16x16x32_bf16 v[64:67], v[196:199], v[148:151], v[64:67]\n"
      "ds_read_b64_tr_b16 v[186:187], v215 offset:18176\n"
      "v_mfma_f32_16x16x32_bf16 v[68:71], v[196:199], v[156:159], v[68:71]\n"
      "ds_read_b64_tr_b16 v[188:189], v215 offset:30720\n"
      "v_mfma_f32_16x16x32_bf16 v[68:71], v[192:195], v[152:155], v[68:71]\n"
      "ds_read_b64_tr_b16 v[190:191], v215 offset:32512\n"
      "v_mfma_f32_16x16x32_bf16 v[72:75], v[192:195], v[160:163], v[72:75]\n"
      "ds_read_b128 v[124:127], v209 offset:2048\n"
      "v_mfma_f32_16x16x32_bf16 v[72:75], v[196:199], v[164:167], v[72:75]\n"
      "ds_read_b128 v[120:123], v208 offset:2048\n"
      "v_mfma_f32_16x16x32_bf16 v[76:79], v[196:199], v[172:175], v[76:79]\n"
      "ds_read_b128 v[128:131], v210 offset:0\n"
      "v_mfma_f32_16x16x32_bf16 v[76:79], v[192:195], v[168:171], v[76:79]\n"
      "ds_read_b128 v[132:135], v211 offset:0\n"
      "v_mfma_f32_16x16x32_bf16 v[80:83], v[200:203], v[144:147], v[80:83]\n"
      "ds_read_b64_tr_b16 v[192:193], v212 offset:16384\n"
      "v_mfma_f32_16x16x32_bf16 v[80:83], v[204:207], v[148:151], v[80:83]\n"
      "ds_read_b64_tr_b16 v[194:195], v212 offset:18176\n"
      "v_mfma_f32_16x16x32_bf16 v[84:87], v[204:207], v[156:159], v[84:87]\n"
      "ds_read_b64_tr_b16 v[196:197], v212 offset:30720\n"
      "v_mfma_f32_16x16x32_bf16 v[84:87], v[200:203], v[152:155], v[84:87]\n"
      "ds_read_b64_tr_b16 v[198:199], v212 offset:32512\n"
      "v_mfma_f32_16x16x32_bf16 v[88:91], v[200:203], v[160:163], v[88:91]\n"
      "ds_read_b128 v[140:143], v211 offset:2048\n"
      "v_mfma_f32_16x16x32_bf16 v[88:91], v[204:207], v[164:167], v[88:91]\n"
      "ds_read_b128 v[136:139], v210 offset:2048\n"
      "v_mfma_f32_16x16x32_bf16 v[92:95], v[204:207], v[172:175], v[92:95]\n"
      "v_mfma_f32_16x16x32_bf16 v[92:95], v[200:203], v[168:171], v[92:95]\n"
      ".Lk_g1:\n"
      "s_waitcnt lgkmcnt(6)\n"
      "v_mfma_f32_16x16x32_bf16 v[96:99], v[176:179], v[112:115], v[96:99]\n"
      "ds_read_b64_tr_b16 v[200:201], v213 offset:16384\n"
      "v_mfma_f32_16x16x32_bf16 v[96:99], v[180:183], v[116:119], v[96:99]\n"
      "ds_read_b64_tr_b16 v[202:203], v213 offset:18176\n"
      "v_mfma_f32_16x16x32_bf16 v[100:103], v[180:183], v[124:127], v[100:103]\n"
      "ds_read_b64_tr_b16 v[204:205], v213 offset:30720\n"
      "v_mfma_f32_16x16x32_bf16 v[100:103], v[176:179], v[120:123], v[100:103]\n"
      "ds_read_b64_tr_b16 v[206:207], v213 offset:32512\n"
      "v_mfma_f32_16x16x32_bf16 v[104:107], v[184:187], v[112:115], v[104:107]\n"
      "ds_read_b64_tr_b16 v[176:177], v212 offset:16512\n"
      "v_mfma_f32_16x16x32_bf16 v[104:107], v[188:191], v[116:119], v[104:107]\n"
      "ds_read_b64_tr_b16 v[178:179], v212 offset:18304\n"
      "v_mfma_f32_16x16x32_bf16 v[108:111], v[188:191], v[124:127], v[108:111]\n"
      "ds_read_b64_tr_b16 v[180:181], v212 offset:30848\n"
      "s_cmp_gt_u32 s60, 5\n"
      "v_mfma_f32_16x16x32_bf16 v[108:111], v[184:187], v[120:123], v[108:111]\n"
      "ds_read_b64_tr_b16 v[182:183], v212 offset:32640\n"
      "s_cbranch_scc0 .Lk_sq0\n"
      "s_sleep 3\n"
      ".Lk_sq0:\n"
      "s_waitcnt lgkmcnt(8)\n"
      "v_mfma_f32_16x16x32_bf16 v[0:3], v[192:195], v[112:115], v[0:3]\n"
      "ds_read_b64_tr_b16 v[184:185], v213 offset:16512\n"
      "v_mfma_f32_16x16x32_bf16 v[0:3], v[196:199], v[116:119], v[0:3]\n"
      "ds_read_b64_tr_b16 v[186:187], v213 offset:18304\n"
      "v_mfma_f32_16x16x32_bf16 v[4:7], v[196:199], v[124:127], v[4:7]\n"
      "ds_read_b64_tr_b16 v[188:189], v213 offset:30848\n"
      "v_mfma_f32_16x16x32_bf16 v[4:7], v[192:195], v[120:123], v[4:7]\n"
      "ds_read_b64_tr_b16 v[190:191], v213 offset:32640\n"
      "v_mfma_f32_16x16x32_bf16 v[8:11], v[192:195], v[128:131], v[8:11]\n"
      "s_add_u32 s61, s61, 1\n"
      "v_mfma_f32_16x16x32_bf16 v[8:11], v[196:199], v[132:135], v[8:11]\n"
      "s_cmp_eq_u32 s61, 3\n"
      "v_mfma_f32_16x16x32_bf16 v[12:15], v[196:199], v[140:143], v[12:15]\n"
      "s_cselect_b32 s62, s64, s63\n"
      "v_mfma_f32_16x16x32_bf16 v[12:15], v[192:195], v[136:139], v[12:15]\n"
      "s_cselect_b32 s61, 0, s61\n"
      "s_waitcnt lgkmcnt(4)\n"
      "v_mfma_f32_16x16x32_bf16 v[16:19], v[200:203], v[112:115], v[16:19]\n"
      "ds_read_b64_tr_b16 v[192:193], v212 offset:16640\n"
      "v_mfma_f32_16x16x32_bf16 v[16:19], v[204:207], v[116:119], v[16:19]\n"
      "ds_read_b64_tr_b16 v[194:195], v212 offset:18432\n"
      "v_mfma_f32_16x16x32_bf16 v[20:23], v[204:207], v[124:127], v[20:23]\n"
      "ds_read_b64_tr_b16 v[196:197], v212 offset:30976\n"
      "v_mfma_f32_16x16x32_bf16 v[20:23], v[200:203], v[120:123], v[20:23]\n"
      "ds_read_b64_tr_b16 v[198:199], v212 offset:32768\n"
      "v_mfma_f32_16x16x32_bf16 v[24:27], v[200:203], v[128:131], v[24:27]\n"
      "v_add_u32 v208, s62, v208\n"
      "v_mfma_f32_16x16x32_bf16 v[24:27], v[204:207], v[132:135], v[24:27]\n"
      "v_add_u32 v209, s62, v209\n"
      "v_mfma_f32_16x16x32_bf16 v[28:31], v[204:207], v[140:143], v[28:31]\n"
      "v_add_u32 v210, s62, v210\n"
      "s_sleep 1\n"
      "v_mfma_f32_16x16x32_bf16 v[28:31], v[200:203], v[136:139], v[28:31]\n"
      "v_add_u32 v211, s62, v211\n"
      "v_mfma_f32_16x16x32_bf16 v[32:35], v[176:179], v[112:115], v[32:35]\n"
      "ds_read_b64_tr_b16 v[200:201], v213 offset:16640\n"
      "v_mfma_f32_16x16x32_bf16 v[32:35], v[180:183], v[116:119], v[32:35]\n"
      "ds_read_b64_tr_b16 v[202:203], v213 offset:18432\n"
      "v_mfma_f32_16x16x32_bf16 v[36:39], v[180:183], v[124:127], v[36:39]\n"
      "ds_read_b64_tr_b16 v[204:205], v213 offset:30976\n"
      "v_mfma_f32_16x16x32_bf16 v[36:39], v[176:179], v[120:123], v[36:39]\n"
      "ds_read_b64_tr_b16 v[206:207], v213 offset:32768\n"
      "v_mfma_f32_16x16x32_bf16 v[40:43], v[176:179], v[128:131], v[40:43]\n"
      "v_add_u32 v212, s62, v212\n"
      "v_mfma_f32_16x16x32_bf16 v[40:43], v[180:183], v[132:135], v[40:43]\n"
      "v_add_u32 v213, s62, v213\n"
      "v_mfma_f32_16x16x32_bf16 v[44:47], v[180:183], v[140:143], v[44:47]\n"
      "v_add_u32 v214, s62, v214\n"
      "v_mfma_f32_16x16x32_bf16 v[44:47], v[176:179], v[136:139], v[44:47]\n"
      "v_add_u32 v215, s62, v215\n"
      "s_waitcnt lgkmcnt(4)\n"
      "v_mfma_f32_16x16x32_bf16 v[48:51], v[184:187], v[112:115], v[48:51]\n"
      "v_mfma_f32_16x16x32_bf16 v[48:51], v[188:191], v[116:119], v[48:51]\n"
      "s_waitcnt lgkmcnt(0)\n"
      "s_barrier\n"
      "v_mfma_f32_16x16x32_bf16 v[52:55], v[188:191], v[124:127], v[52:55]\n"
      "ds_read_b64_tr_b16 v[176:177], v214 offset:16384\n"
      "v_mfma_f32_16x16x32_bf16 v[52:55], v[184:187], v[120:123], v[52:55]\n"
      "ds_read_b64_tr_b16 v[178:179], v214 offset:18176\n"
      "v_mfma_f32_16x16x32_bf16 v[56:59], v[184:187], v[128:131], v[56:59]\n"
      "ds_read_b64_tr_b16 v[180:181], v214 offset:30720\n"
      "v_mfma_f32_16x16x32_bf16 v[56:59], v[188:191], v[132:135], v[56:59]\n"
      "ds_read_b64_tr_b16 v[182:183], v214 offset:32512\n"
      "v_mfma_f32_16x16x32_bf16 v[60:63], v[188:191], v[140:143], v[60:63]\n"
      "ds_read_b128 v[144:147], v208 offset:0\n"
      "v_mfma_f32_16x16x32_bf16 v[60:63], v[184:187], v[136:139], v[60:63]\n"
      "ds_read_b128 v[148:151], v209 offset:0\n"
      "v_mfma_f32_16x16x32_bf16 v[64:67], v[192:195], v[112:115], v[64:67]\n"
      "ds_read_b64_tr_b16 v[184:185], v215 offset:16384\n"
      "v_mfma_f32_16x16x32_bf16 v[64:67], v[196:199], v[116:119], v[64:67]\n"
      "ds_read_b64_tr_b16 v[186:187], v215 offset:18176\n"
      "v_mfma_f32_16x16x32_bf16 v[68:71], v[196:199], v[124:127], v[68:71]\n"
      "ds_read_b64_tr_b16 v[188:189], v215 offset:30720\n"
      "v_mfma_f32_16x16x32_bf16 v[68:71], v[192:195], v[120:123], v[68:71]\n"
      "ds_read_b64_tr_b16 v[190:191], v215 offset:32512\n"
      "v_mfma_f32_16x16x32_bf16 v[72:75], v[192:195], v[128:131], v[72:75]\n"
      "ds_read_b128 v[156:159], v209 offset:2048\n"
      "v_mfma_f32_16x16x32_bf16 v[72:75], v[196:199], v[132:135], v[72:75]\n"
      "ds_read_b128 v[152:155], v208 offset:2048\n"
      "v_mfma_f32_16x16x32_bf16 v[76:79], v[196:199], v[140:143], v[76:79]\n"
      "ds_read_b128 v[160:163], v210 offset:0\n"
      "v_mfma_f32_16x16x32_bf16 v[76:79], v[192:195], v[136:139], v[76:79]\n"
      "ds_read_b128 v[164:167], v211 offset:0\n"
      "v_mfma_f32_16x16x32_bf16 v[80:83], v[200:203], v[112:115], v[80:83]\n"
      "ds_read_b64_tr_b16 v[192:193], v212 offset:16384\n"
      "v_mfma_f32_16x16x32_bf16 v[80:83], v[204:207], v[116:119], v[80:83]\n"
      "ds_read_b64_tr_b16 v[194:195], v212 offset:18176\n"
      "v_mfma_f32_16x16x32_bf16 v[84:87], v[204:207], v[124:127], v[84:87]\n"
      "ds_read_b64_tr_b16 v[196:197], v212 offset:30720\n"
      "v_mfma_f32_16x16x32_bf16 v[84:87], v[200:203], v[120:123], v[84:87]\n"
      "ds_read_b64_tr_b16 v[198:199], v212 offset:32512\n"
      "v_mfma_f32_16x16x32_bf16 v[88:91], v[200:203], v[128:131], v[88:91]\n"
      "ds_read_b128 v[172:175], v211 offset:2048\n"
      "v_mfma_f32_16x16x32_bf16 v[88:91], v[204:207], v[132:135], v[88:91]\n"
      "ds_read_b128 v[168:171], v210 offset:2048\n"
      "v_mfma_f32_16x16x32_bf16 v[92:95], v[204:207], v[140:143], v[92:95]\n"
      "v_mfma_f32_16x16x32_bf16 v[92:95], v[200:203], v[136:139], v[92:95]\n"
      "s_waitcnt lgkmcnt(6)\n"
      "v_mfma_f32_16x16x32_bf16 v[96:99], v[176:179], v[144:147], v[96:99]\n"
      "ds_read_b64_tr_b16 v[200:201], v213 offset:16384\n"
      "v_mfma_f32_16x16x32_bf16 v[96:99], v[180:183], v[148:151], v[96:99]\n"
      "ds_read_b64_tr_b16 v[202:203], v213 offset:18176\n"
      "v_mfma_f32_16x16x32_bf16 v[100:103], v[180:183], v[156:159], v[100:103]\n"
      "ds_read_b64_tr_b16 v[204:205], v213 offset:30720\n"
      "v_mfma_f32_16x16x32_bf16 v[100:103], v[176:179], v[152:155], v[100:103]\n"
      "ds_read_b64_tr_b16 v[206:207], v213 offset:32512\n"
      "v_mfma_f32_16x16x32_bf16 v[104:107], v[184:187], v[144:147], v[104:107]\n"
      "ds_read_b64_tr_b16 v[176:177], v212 offset:16512\n"
      "v_mfma_f32_16x16x32_bf16 v[104:107], v[188:191], v[148:151], v[104:107]\n"
      "ds_read_b64_tr_b16 v[178:179], v212 offset:18304\n"
      "v_mfma_f32_16x16x32_bf16 v[108:111], v[188:191], v[156:159], v[108:111]\n"
      "ds_read_b64_tr_b16 v[180:181], v212 offset:30848\n"
      "s_cmp_gt_u32 s60, 5\n"
      "v_mfma_f32_16x16x32_bf16 v[108:111], v[184:187], v[152:155], v[108:111]\n"
      "ds_read_b64_tr_b16 v[182:183], v212 offset:32640\n"
      "s_cbranch_scc0 .Lk_sq1\n"
      "s_sleep 3\n"
      ".Lk_sq1:\n"
      "s_waitcnt lgkmcnt(8)\n"
      "v_mfma_f32_16x16x32_bf16 v[0:3], v[192:195], v[144:147], v[0:3]\n"
      "ds_read_b64_tr_b16 v[184:185], v213 offset:16512\n"
      "v_mfma_f32_16x16x32_bf16 v[0:3], v[196:199], v[148:151], v[0:3]\n"
      "ds_read_b64_tr_b16 v[186:187], v213 offset:18304\n"
      "v_mfma_f32_16x16x32_bf16 v[4:7], v[196:199], v[156:159], v[4:7]\n"
      "ds_read_b64_tr_b16 v[188:189], v213 offset:30848\n"
      "v_mfma_f32_16x16x32_bf16 v[4:7], v[192:195], v[152:155], v[4:7]\n"
      "ds_read_b64_tr_b16 v[190:191], v213 offset:32640\n"
      "v_mfma_f32_16x16x32_bf16 v[8:11], v[192:195], v[160:163], v[8:11]\n"
      "s_add_u32 s61, s61, 1\n"
      "v_mfma_f32_16x16x32_bf16 v[8:11], v[196:199], v[164:167], v[8:11]\n"
      "s_cmp_eq_u32 s61, 3\n"
      "v_mfma_f32_16x16x32_bf16 v[12:15], v[196:199], v[172:175], v[12:15]\n"
      "s_cselect_b32 s62, s64, s63\n"
      "v_mfma_f32_16x16x32_bf16 v[12:15], v[192:195], v[168:171], v[12:15]\n"
      "s_cselect_b32 s61, 0, s61\n"
      "s_waitcnt lgkmcnt(4)\n"
      "v_mfma_f32_16x16x32_bf16 v[16:19], v[200:203], v[144:147], v[16:19]\n"
      "ds_read_b64_tr_b16 v[192:193], v212 offset:16640\n"
      "v_mfma_f32_16x16x32_bf16 v[16:19], v[204:207], v[148:151], v[16:19]\n"
      "ds_read_b64_tr_b16 v[194:195], v212 offset:18432\n"
      "v_mfma_f32_16x16x32_bf16 v[20:23], v[204:207], v[156:159], v[20:23]\n"
      "ds_read_b64_tr_b16 v[196:197], v212 offset:30976\n"
      "v_mfma_f32_16x16x32_bf16 v[20:23], v[200:203], v[152:155], v[20:23]\n"
      "ds_read_b64_tr_b16 v[198:199], v212 offset:32768\n"
      "v_mfma_f32_16x16x32_bf16 v[24:27], v[200:203], v[160:163], v[24:27]\n"
      "v_add_u32 v208, s62, v208\n"
      "v_mfma_f32_16x16x32_bf16 v[24:27], v[204:207], v[164:167], v[24:27]\n"
      "v_add_u32 v209, s62, v209\n"
      "v_mfma_f32_16x16x32_bf16 v[28:31], v[204:207], v[172:175], v[28:31]\n"
      "v_add_u32 v210, s62, v210\n"
      "s_sleep 1\n"
      "v_mfma_f32_16x16x32_bf16 v[28:31], v[200:203], v[168:171], v[28:31]\n"
      "v_add_u32 v211, s62, v211\n"
      "v_mfma_f32_16x16x32_bf16 v[32:35], v[176:179], v[144:147], v[32:35]\n"
      "ds_read_b64_tr_b16 v[200:201], v213 offset:16640\n"
      "v_mfma_f32_16x16x32_bf16 v[32:35], v[180:183], v[148:151], v[32:35]\n"
      "ds_read_b64_tr_b16 v[202:203], v213 offset:18432\n"
      "v_mfma_f32_16x16x32_bf16 v[36:39], v[180:183], v[156:159], v[36:39]\n"
      "ds_read_b64_tr_b16 v[204:205], v213 offset:30976\n"
      "v_mfma_f32_16x16x32_bf16 v[36:39], v[176:179], v[152:155], v[36:39]\n"
      "ds_read_b64_tr_b16 v[206:207], v213 offset:32768\n"
      "v_mfma_f32_16x16x32_bf16 v[40:43], v[176:179], v[160:163], v[40:43]\n"
      "v_add_u32 v212, s62, v212\n"
      "v_mfma_f32_16x16x32_bf16 v[40:43], v[180:183], v[164:167], v[40:43]\n"
      "v_add_u32 v213, s62, v213\n"
      "v_mfma_f32_16x16x32_bf16 v[44:47], v[180:183], v[172:175], v[44:47]\n"
      "v_add_u32 v214, s62, v214\n"
      "v_mfma_f32_16x16x32_bf16 v[44:47], v[176:179], v[168:171], v[44:47]\n"
      "v_add_u32 v215, s62, v215\n"
      "s_waitcnt lgkmcnt(4)\n"
      "v_mfma_f32_16x16x32_bf16 v[48:51], v[184:187], v[144:147], v[48:51]\n"
      "s_waitcnt vmcnt(0)\n"
      "v_mfma_f32_16x16x32_bf16 v[48:51], v[188:191], v[148:151], v[48:51]\n"
      "s_waitcnt lgkmcnt(0)\n"
      "s_barrier\n"
      "v_mfma_f32_16x16x32_bf16 v[52:55], v[188:191], v[156:159], v[52:55]\n"
      "ds_read_b64_tr_b16 v[176:177], v214 offset:16384\n"
      "v_mfma_f32_16x16x32_bf16 v[52:55], v[184:187], v[152:155], v[52:55]\n"
      "ds_read_b64_tr_b16 v[178:179], v214 offset:18176\n"
      "v_mfma_f32_16x16x32_bf16 v[56:59], v[184:187], v[160:163], v[56:59]\n"
      "ds_read_b64_tr_b16 v[180:181], v214 offset:30720\n"
      "v_mfma_f32_16x16x32_bf16 v[56:59], v[188:191], v[164:167], v[56:59]\n"
      "ds_read_b64_tr_b16 v[182:183], v214 offset:32512\n"
      "v_mfma_f32_16x16x32_bf16 v[60:63], v[188:191], v[172:175], v[60:63]\n"
      "ds_read_b128 v[112:115], v208 offset:0\n"
      "v_mfma_f32_16x16x32_bf16 v[60:63], v[184:187], v[168:171], v[60:63]\n"
      "ds_read_b128 v[116:119], v209 offset:0\n"
      "v_mfma_f32_16x16x32_bf16 v[64:67], v[192:195], v[144:147], v[64:67]\n"
      "ds_read_b64_tr_b16 v[184:185], v215 offset:16384\n"
      "v_mfma_f32_16x16x32_bf16 v[64:67], v[196:199], v[148:151], v[64:67]\n"
      "ds_read_b64_tr_b16 v[186:187], v215 offset:18176\n"
      "v_mfma_f32_16x16x32_bf16 v[68:71], v[196:199], v[156:159], v[68:71]\n"
      "ds_read_b64_tr_b16 v[188:189], v215 offset:30720\n"
      "v_mfma_f32_16x16x32_bf16 v[68:71], v[192:195], v[152:155], v[68:71]\n"
      "ds_read_b64_tr_b16 v[190:191], v215 offset:32512\n"
      "v_mfma_f32_16x16x32_bf16 v[72:75], v[192:195], v[160:163], v[72:75]\n"
      "ds_read_b128 v[124:127], v209 offset:2048\n"
      "v_mfma_f32_16x16x32_bf16 v[72:75], v[196:199], v[164:167], v[72:75]\n"
      "ds_read_b128 v[120:123], v208 offset:2048\n"
      "v_mfma_f32_16x16x32_bf16 v[76:79], v[196:199], v[172:175], v[76:79]\n"
      "ds_read_b128 v[128:131], v210 offset:0\n"
      "v_mfma_f32_16x16x32_bf16 v[76:79], v[192:195], v[168:171], v[76:79]\n"
      "ds_read_b128 v[132:135], v211 offset:0\n"
      "v_mfma_f32_16x16x32_bf16 v[80:83], v[200:203], v[144:147], v[80:83]\n"
      "ds_read_b64_tr_b16 v[192:193], v212 offset:16384\n"
      "v_mfma_f32_16x16x32_bf16 v[80:83], v[204:207], v[148:151], v[80:83]\n"
      "ds_read_b64_tr_b16 v[194:195], v212 offset:18176\n"
      "v_mfma_f32_16x16x32_bf16 v[84:87], v[204:207], v[156:159], v[84:87]\n"
      "ds_read_b64_tr_b16 v[196:197], v212 offset:30720\n"
      "v_mfma_f32_16x16x32_bf16 v[84:87], v[200:203], v[152:155], v[84:87]\n"
      "ds_read_b64_tr_b16 v[198:199], v212 offset:32512\n"
      "v_mfma_f32_16x16x32_bf16 v[88:91], v[200:203], v[160:163], v[88:91]\n"
      "ds_read_b128 v[140:143], v211 offset:2048\n"
      "v_mfma_f32_16x16x32_bf16 v[88:91], v[204:207], v[164:167], v[88:91]\n"
      "ds_read_b128 v[136:139], v210 offset:2048\n"
      "s_sub_u32 s60, s60, 1\n"
      "v_mfma_f32_16x16x32_bf16 v[92:95], v[204:207], v[172:175], v[92:95]\n"
      "s_cmp_lg_u32 s60, 0\n"
      "v_mfma_f32_16x16x32_bf16 v[92:95], v[200:203], v[168:171], v[92:95]\n"
      "s_cbranch_scc1 .Lk_g1\n"
      "s_waitcnt lgkmcnt(6)\n"
      "v_mfma_f32_16x16x32_bf16 v[96:99], v[176:179], v[112:115], v[96:99]\n"
      "ds_read_b64_tr_b16 v[200:201], v213 offset:16384\n"
      "v_readfirstlane_b32 s48, v248\n"
      "v_mfma_f32_16x16x32_bf16 v[96:99], v[180:183], v[116:119], v[96:99]\n"
      "ds_read_b64_tr_b16 v[202:203], v213 offset:18176\n"
      "v_readfirstlane_b32 s49, v249\n"
      "v_mfma_f32_16x16x32_bf16 v[100:103], v[180:183], v[124:127], v[100:103]\n"
      "ds_read_b64_tr_b16 v[204:205], v213 offset:30720\n"
      "s_mul_i32 s50, s48, 0x9e3779b9\n"
      "v_mfma_f32_16x16x32_bf16 v[100:103], v[176:179], v[120:123], v[100:103]\n"
      "ds_read_b64_tr_b16 v[206:207], v213 offset:32512\n"
      "s_mul_hi_u32 s51, s48, 0x7f4a7c15\n"
      "v_mfma_f32_16x16x32_bf16 v[104:107], v[184:187], v[112:115], v[104:107]\n"
      "ds_read_b64_tr_b16 v[176:177], v212 offset:16512\n"
      "s_mul_i32 s52, s49, 0x7f4a7c15\n"
      "v_mfma_f32_16x16x32_bf16 v[104:107], v[188:191], v[116:119], v[104:107]\n"
      "ds_read_b64_tr_b16 v[178:179], v212 offset:18304\n"
      "s_add_u32 s50, s50, s51\n"
      "v_mfma_f32_16x16x32_bf16 v[108:111], v[188:191], v[124:127], v[108:111]\n"
      "ds_read_b64_tr_b16 v[180:181], v212 offset:30848\n"
      "s_add_u32 s50, s50, s52\n"
      "v_mfma_f32_16x16x32_bf16 v[108:111], v[184:187], v[120:123], v[108:111]\n"
      "ds_read_b64_tr_b16 v[182:183], v212 offset:32640\n"
      "s_mul_i32 s48, s48, 0x7f4a7c15\n"
      "s_waitcnt lgkmcnt(8)\n"
      "v_mfma_f32_16x16x32_bf16 v[0:3], v[192:195], v[112:115], v[0:3]\n"
      "ds_read_b64_tr_b16 v[184:185], v213 offset:16512\n"
      "s_add_u32 s48, s48, 1\n"
      "v_mfma_f32_16x16x32_bf16 v[0:3], v[196:199], v[116:119], v[0:3]\n"
      "ds_read_b64_tr_b16 v[186:187], v213 offset:18304\n"
      "s_addc_u32 s49, s50, 0\n"
      "v_mfma_f32_16x16x32_bf16 v[4:7], v[196:199], v[124:127], v[4:7]\n"
      "ds_read_b64_tr_b16 v[188:189], v213 offset:30848\n"
      "v_mov_b32 v248, s48\n"
      "v_mfma_f32_16x16x32_bf16 v[4:7], v[192:195], v[120:123], v[4:7]\n"
      "ds_read_b64_tr_b16 v[190:191], v213 offset:32640\n"
      "v_mfma_f32_16x16x32_bf16 v[8:11], v[192:195], v[128:131], v[8:11]\n"
      "s_add_u32 s61, s61, 1\n"
      "v_mfma_f32_16x16x32_bf16 v[8:11], v[196:199], v[132:135], v[8:11]\n"
      "s_cmp_eq_u32 s61, 3\n"
      "v_mfma_f32_16x16x32_bf16 v[12:15], v[196:199], v[140:143], v[12:15]\n"
      "s_cselect_b32 s62, s64, s63\n"
      "v_mfma_f32_16x16x32_bf16 v[12:15], v[192:195], v[136:139], v[12:15]\n"
      "s_cselect_b32 s61, 0, s61\n"
      "s_waitcnt lgkmcnt(4)\n"
      "v_mfma_f32_16x16x32_bf16 v[16:19], v[200:203], v[112:115], v[16:19]\n"
      "ds_read_b64_tr_b16 v[192:193], v212 offset:16640\n"
      "v_mov_b32 v249, s49\n"
      "v_mfma_f32_16x16x32_bf16 v[16:19], v[204:207], v[116:119], v[16:19]\n"
      "ds_read_b64_tr_b16 v[194:195], v212 offset:18432\n"
      "s_bfe_u32 s83, s24, 0x20001\n"
      "v_mfma_f32_16x16x32_bf16 v[20:23], v[204:207], v[124:127], v[20:23]\n"
      "ds_read_b64_tr_b16 v[196:197], v212 offset:30976\n"
      "s_mul_i32 s83, s83, 7\n"
      "v_mfma_f32_16x16x32_bf16 v[20:23], v[200:203], v[120:123], v[20:23]\n"
      "ds_read_b64_tr_b16 v[198:199], v212 offset:32768\n"
      "s_add_u32 s83, s83, 5\n"
      "v_mfma_f32_16x16x32_bf16 v[24:27], v[200:203], v[128:131], v[24:27]\n"
      "v_add_u32 v208, s62, v208\n"
      "s_lshl_b32 s83, s83, 4\n"
      "v_mfma_f32_16x16x32_bf16 v[24:27], v[204:207], v[132:135], v[24:27]\n"
      "v_add_u32 v209, s62, v209\n"
      "s_and_b32 s50, s24, 1\n"
      "v_mfma_f32_16x16x32_bf16 v[28:31], v[204:207], v[140:143], v[28:31]\n"
      "v_add_u32 v210, s62, v210\n"
      "s_add_u32 s83, s83, s50\n"
      "v_mfma_f32_16x16x32_bf16 v[28:31], v[200:203], v[136:139], v[28:31]\n"
      "v_add_u32 v211, s62, v211\n"
      "s_lshr_b32 s50, s24, 3\n"
      "v_mfma_f32_16x16x32_bf16 v[32:35], v[176:179], v[112:115], v[32:35]\n"
      "ds_read_b64_tr_b16 v[200:201], v213 offset:16640\n"
      "s_lshl_b32 s50, s50, 1\n"
      "v_mfma_f32_16x16x32_bf16 v[32:35], v[180:183], v[116:119], v[32:35]\n"
      "ds_read_b64_tr_b16 v[202:203], v213 offset:18432\n"
      "s_add_u32 s83, s83, s50\n"
      "v_mfma_f32_16x16x32_bf16 v[36:39], v[180:183], v[124:127], v[36:39]\n"
      "ds_read_b64_tr_b16 v[204:205], v213 offset:30976\n"
      "s_lshl_b32 s83, s83, 13\n"
      "v_mfma_f32_16x16x32_bf16 v[36:39], v[176:179], v[120:123], v[36:39]\n"
      "ds_read_b64_tr_b16 v[206:207], v213 offset:32768\n"
      "v_bfe_u32 v245, v250, 2, 2\n"
      "v_mfma_f32_16x16x32_bf16 v[40:43], v[176:179], v[128:131], v[40:43]\n"
      "v_add_u32 v212, s62, v212\n"
      "v_xor_b32 v245, v245, v252\n"
      "v_mfma_f32_16x16x32_bf16 v[40:43], v[180:183], v[132:135], v[40:43]\n"
      "v_add_u32 v213, s62, v213\n"
      "v_and_b32 v246, 48, v253\n"
      "v_mfma_f32_16x16x32_bf16 v[44:47], v[180:183], v[140:143], v[44:47]\n"
      "v_add_u32 v214, s62, v214\n"
      "v_lshl_or_b32 v245, v245, 6, v246\n"
      "v_mfma_f32_16x16x32_bf16 v[44:47], v[176:179], v[136:139], v[44:47]\n"
      "v_add_u32 v215, s62, v215\n"
      "v_lshl_or_b32 v245, v252, 11, v245\n"
      "s_waitcnt lgkmcnt(4)\n"
      "v_mfma_f32_16x16x32_bf16 v[48:51], v[184:187], v[112:115], v[48:51]\n"
      "s_lshl_b32 s50, s29, 15\n"
      "v_mfma_f32_16x16x32_bf16 v[48:51], v[188:191], v[116:119], v[48:51]\n"
      "s_waitcnt lgkmcnt(0)\n"
      "s_barrier\n"
      "v_mfma_f32_16x16x32_bf16 v[52:55], v[188:191], v[124:127], v[52:55]\n"
      "ds_read_b64_tr_b16 v[176:177], v214 offset:16384\n"
      "s_mul_i32 s51, s27, 3670016\n"
      "v_mfma_f32_16x16x32_bf16 v[52:55], v[184:187], v[120:123], v[52:55]\n"
      "ds_read_b64_tr_b16 v[178:179], v214 offset:18176\n"
      "s_add_u32 s50, s50, s51\n"
      "v_mfma_f32_16x16x32_bf16 v[56:59], v[184:187], v[128:131], v[56:59]\n"
      "ds_read_b64_tr_b16 v[180:181], v214 offset:30720\n"
      "s_lshl_b32 s51, s28, 8\n"
      "v_mfma_f32_16x16x32_bf16 v[56:59], v[188:191], v[132:135], v[56:59]\n"
      "ds_read_b64_tr_b16 v[182:183], v214 offset:32512\n"
      "s_add_u32 s50, s50, s51\n"
      "v_mfma_f32_16x16x32_bf16 v[60:63], v[188:191], v[140:143], v[60:63]\n"
      "ds_read_b128 v[144:147], v208 offset:0\n"
      "v_add_u32 v236, s50, v245\n"
      "v_mfma_f32_16x16x32_bf16 v[60:63], v[184:187], v[136:139], v[60:63]\n"
      "ds_read_b128 v[148:151], v209 offset:0\n"
      "v_add_u32 v237, 7160, v236\n"
      "v_mfma_f32_16x16x32_bf16 v[64:67], v[192:195], v[112:115], v[64:67]\n"
      "ds_read_b64_tr_b16 v[184:185], v215 offset:16384\n"
      "v_add_u32 v238, 14320, v236\n"
      "v_mfma_f32_16x16x32_bf16 v[64:67], v[196:199], v[116:119], v[64:67]\n"
      "ds_read_b64_tr_b16 v[186:187], v215 offset:18176\n"
      "v_add_u32 v239, 21480, v236\n"
      "v_mfma_f32_16x16x32_bf16 v[68:71], v[196:199], v[124:127], v[68:71]\n"
      "ds_read_b64_tr_b16 v[188:189], v215 offset:30720\n"
      "s_mul_i32 s77, s29, 4128\n"
      "v_mfma_f32_16x16x32_bf16 v[68:71], v[192:195], v[120:123], v[68:71]\n"
      "ds_read_b64_tr_b16 v[190:191], v215 offset:32512\n"
      "s_and_b32 s52, s24, 7\n"
      "v_mfma_f32_16x16x32_bf16 v[72:75], v[192:195], v[128:131], v[72:75]\n"
      "ds_read_b128 v[156:159], v209 offset:2048\n"
      "s_and_b32 s50, s24, 7\n"
      "v_mfma_f32_16x16x32_bf16 v[72:75], v[196:199], v[132:135], v[72:75]\n"
      "ds_read_b128 v[152:155], v208 offset:2048\n"
      "s_lshr_b32 s51, s24, 3\n"
      "v_mfma_f32_16x16x32_bf16 v[76:79], v[196:199], v[140:143], v[76:79]\n"
      "ds_read_b128 v[160:163], v210 offset:0\n"
      "s_mul_i32 s50, s50, 5\n"
      "v_mfma_f32_16x16x32_bf16 v[76:79], v[192:195], v[136:139], v[76:79]\n"
      "ds_read_b128 v[164:167], v211 offset:0\n"
      "s_add_u32 s50, s50, s51\n"
      "v_mfma_f32_16x16x32_bf16 v[80:83], v[200:203], v[112:115], v[80:83]\n"
      "ds_read_b64_tr_b16 v[192:193], v212 offset:16384\n"
      "s_add_u32 s50, s50, s51\n"
      "v_mfma_f32_16x16x32_bf16 v[80:83], v[204:207], v[116:119], v[80:83]\n"
      "ds_read_b64_tr_b16 v[194:195], v212 offset:18176\n"
      "s_lshr_b32 s50, s50, 1\n"
      "v_mfma_f32_16x16x32_bf16 v[84:87], v[204:207], v[124:127], v[84:87]\n"
      "ds_read_b64_tr_b16 v[196:197], v212 offset:30720\n"
      "s_sub_u32 s50, s50, 8\n"
      "v_mfma_f32_16x16x32_bf16 v[84:87], v[200:203], v[120:123], v[84:87]\n"
      "ds_read_b64_tr_b16 v[198:199], v212 offset:32512\n"
      "v_add_u32 v245, s52, v250\n"
      "v_mfma_f32_16x16x32_bf16 v[88:91], v[200:203], v[128:131], v[88:91]\n"
      "ds_read_b128 v[172:175], v211 offset:2048\n"
      "v_and_b32 v245, 7, v245\n"
      "v_mfma_f32_16x16x32_bf16 v[88:91], v[204:207], v[132:135], v[88:91]\n"
      "ds_read_b128 v[168:171], v210 offset:2048\n"
      "v_lshrrev_b32 v246, 1, v245\n"
      "v_mfma_f32_16x16x32_bf16 v[92:95], v[204:207], v[140:143], v[92:95]\n"
      "v_mad_i32_i24 v241, v246, -5, v245\n"
      "v_mfma_f32_16x16x32_bf16 v[92:95], v[200:203], v[136:139], v[92:95]\n"
      "v_lshrrev_b32 v246, 0, v245\n"
      "s_waitcnt lgkmcnt(6)\n"
      "v_mfma_f32_16x16x32_bf16 v[96:99], v[176:179], v[144:147], v[96:99]\n"
      "ds_read_b64_tr_b16 v[200:201], v213 offset:16384\n"
      "v_mad_i32_i24 v241, v246, 5, v241\n"
      "v_mfma_f32_16x16x32_bf16 v[96:99], v[180:183], v[148:151], v[96:99]\n"
      "ds_read_b64_tr_b16 v[202:203], v213 offset:18176\n"
      "v_add_u32 v245, s50, v250\n"
      "v_mfma_f32_16x16x32_bf16 v[100:103], v[180:183], v[156:159], v[100:103]\n"
      "ds_read_b64_tr_b16 v[204:205], v213 offset:30720\n"
      "v_subrev_u32 v246, 20, v245\n"
      "v_mfma_f32_16x16x32_bf16 v[100:103], v[176:179], v[152:155], v[100:103]\n"
      "ds_read_b64_tr_b16 v[206:207], v213 offset:32512\n"
      "v_min_u32 v245, v245, v246\n"
      "v_mfma_f32_16x16x32_bf16 v[104:107], v[184:187], v[144:147], v[104:107]\n"
      "ds_read_b64_tr_b16 v[176:177], v212 offset:16512\n"
      "v_mul_u32_u24 v246, 13, v245\n"
      "v_mfma_f32_16x16x32_bf16 v[104:107], v[188:191], v[148:151], v[104:107]\n"
      "ds_read_b64_tr_b16 v[178:179], v212 offset:18304\n"
      "v_lshrrev_b32 v246, 6, v246\n"
      "v_mfma_f32_16x16x32_bf16 v[108:111], v[188:191], v[156:159], v[108:111]\n"
      "ds_read_b64_tr_b16 v[180:181], v212 offset:30848\n"
      "v_lshl_add_u32 v245, v246, 1, v245\n"
      "v_mfma_f32_16x16x32_bf16 v[108:111], v[184:187], v[152:155], v[108:111]\n"
      "ds_read_b64_tr_b16 v[182:183], v212 offset:32640\n"
      "v_add_u32 v245, 1, v245\n"
      "s_waitcnt lgkmcnt(8)\n"
      "v_mfma_f32_16x16x32_bf16 v[0:3], v[192:195], v[144:147], v[0:3]\n"
      "ds_read_b64_tr_b16 v[184:185], v213 offset:16512\n"
      "v_cmp_gt_u32 vcc, 8, v250\n"
      "v_mfma_f32_16x16x32_bf16 v[0:3], v[196:199], v[148:151], v[0:3]\n"
      "ds_read_b64_tr_b16 v[186:187], v213 offset:18304\n"
      "v_cndmask_b32 v241, v245, v241, vcc\n"
      "v_mfma_f32_16x16x32_bf16 v[4:7], v[196:199], v[156:159], v[4:7]\n"
      "ds_read_b64_tr_b16 v[188:189], v213 offset:30848\n"
      "v_lshlrev_b32 v241, 17, v241\n"
      "v_mfma_f32_16x16x32_bf16 v[4:7], v[192:195], v[152:155], v[4:7]\n"
      "ds_read_b64_tr_b16 v[190:191], v213 offset:32640\n"
      "s_mul_i32 s51, s27, 3670016\n"
      "v_mfma_f32_16x16x32_bf16 v[8:11], v[192:195], v[160:163], v[8:11]\n"
      "s_add_u32 s52, s51, s75\n"
      "v_mfma_f32_16x16x32_bf16 v[8:11], v[196:199], v[164:167], v[8:11]\n"
      "s_lshl_b32 s50, s29, 12\n"
      "v_mfma_f32_16x16x32_bf16 v[12:15], v[196:199], v[172:175], v[12:15]\n"
      "s_add_u32 s52, s52, s50\n"
      "v_mfma_f32_16x16x32_bf16 v[12:15], v[192:195], v[168:171], v[12:15]\n"
      "v_add_u32 v242, s52, v253\n"
      "s_waitcnt lgkmcnt(4)\n"
      "v_mfma_f32_16x16x32_bf16 v[16:19], v[200:203], v[144:147], v[16:19]\n"
      "ds_read_b64_tr_b16 v[192:193], v212 offset:16640\n"
      "s_add_u32 s65, s50, 16512\n"
      "v_mfma_f32_16x16x32_bf16 v[16:19], v[204:207], v[148:151], v[16:19]\n"
      "ds_read_b64_tr_b16 v[194:195], v212 offset:18432\n"
      "v_and_b32 v244, 7, v250\n"
      "v_mfma_f32_16x16x32_bf16 v[20:23], v[204:207], v[156:159], v[20:23]\n"
      "ds_read_b64_tr_b16 v[196:197], v212 offset:30976\n"
      "v_lshlrev_b32 v244, 3, v244\n"
      "v_mfma_f32_16x16x32_bf16 v[20:23], v[200:203], v[152:155], v[20:23]\n"
      "ds_read_b64_tr_b16 v[198:199], v212 offset:32768\n"
      "v_readlane_b32 s62, v241, 0\n"
      "v_mfma_f32_16x16x32_bf16 v[24:27], v[200:203], v[160:163], v[24:27]\n"
      "v_readlane_b32 s63, v241, 1\n"
      "v_mfma_f32_16x16x32_bf16 v[24:27], v[204:207], v[164:167], v[24:27]\n"
      "v_readlane_b32 s64, v241, 2\n"
      "v_mfma_f32_16x16x32_bf16 v[28:31], v[204:207], v[172:175], v[28:31]\n"
      "v_readlane_b32 s61, v241, 8\n"
      "v_mfma_f32_16x16x32_bf16 v[28:31], v[200:203], v[168:171], v[28:31]\n"
      "v_readlane_b32 s60, v241, 9\n"
      "v_mfma_f32_16x16x32_bf16 v[32:35], v[176:179], v[144:147], v[32:35]\n"
      "ds_read_b64_tr_b16 v[200:201], v213 offset:16640\n"
      "s_lshr_b32 s58, s26, 3\n"
      "v_mfma_f32_16x16x32_bf16 v[32:35], v[180:183], v[148:151], v[32:35]\n"
      "ds_read_b64_tr_b16 v[202:203], v213 offset:18432\n"
      "s_lshl_b32 s58, s58, 8\n"
      "v_mfma_f32_16x16x32_bf16 v[36:39], v[180:183], v[156:159], v[36:39]\n"
      "ds_read_b64_tr_b16 v[204:205], v213 offset:30976\n"
      "s_add_u32 s58, s58, 81920\n"
      "v_mfma_f32_16x16x32_bf16 v[36:39], v[176:179], v[152:155], v[36:39]\n"
      "ds_read_b64_tr_b16 v[206:207], v213 offset:32768\n"
      "s_lshl_b32 s59, s28, 3\n"
      "v_mfma_f32_16x16x32_bf16 v[40:43], v[176:179], v[160:163], v[40:43]\n"
      "s_add_u32 s59, s59, s58\n"
      "v_mfma_f32_16x16x32_bf16 v[40:43], v[180:183], v[164:167], v[40:43]\n"
      "v_mfma_f32_16x16x32_bf16 v[44:47], v[180:183], v[172:175], v[44:47]\n"
      "v_mfma_f32_16x16x32_bf16 v[44:47], v[176:179], v[168:171], v[44:47]\n"
      "s_waitcnt lgkmcnt(4)\n"
      "v_mfma_f32_16x16x32_bf16 v[48:51], v[184:187], v[144:147], v[48:51]\n"
      "v_mfma_f32_16x16x32_bf16 v[48:51], v[188:191], v[148:151], v[48:51]\n"
      "s_waitcnt lgkmcnt(0)\n"
      "s_barrier\n"
      "v_mfma_f32_16x16x32_bf16 v[52:55], v[188:191], v[156:159], v[52:55]\n"
      "ds_read_b128 v[216:219], v232 offset:0\n"
      "v_mfma_f32_16x16x32_bf16 v[52:55], v[184:187], v[152:155], v[52:55]\n"
      "ds_read_b128 v[220:223], v232 offset:256\n"
      "v_mfma_f32_16x16x32_bf16 v[56:59], v[184:187], v[160:163], v[56:59]\n"
      "ds_read_b128 v[224:227], v233 offset:0\n"
      "v_mfma_f32_16x16x32_bf16 v[56:59], v[188:191], v[164:167], v[56:59]\n"
      "ds_read_b128 v[228:231], v233 offset:256\n"
      "v_mfma_f32_16x16x32_bf16 v[60:63], v[188:191], v[172:175], v[60:63]\n"
      "ds_read_b128 v[136:139], v235 offset:0\n"
      "v_mfma_f32_16x16x32_bf16 v[60:63], v[184:187], v[168:171], v[60:63]\n"
      "ds_read_b128 v[140:143], v235 offset:64\n"
      "v_mfma_f32_16x16x32_bf16 v[64:67], v[192:195], v[144:147], v[64:67]\n"
      "ds_read_b128 v[112:115], v234 offset:0\n"
      "v_mfma_f32_16x16x32_bf16 v[64:67], v[196:199], v[148:151], v[64:67]\n"
      "ds_read_b128 v[116:119], v234 offset:64\n"
      "v_mfma_f32_16x16x32_bf16 v[68:71], v[196:199], v[156:159], v[68:71]\n"
      "ds_read_b128 v[120:123], v234 offset:256\n"
      "v_mfma_f32_16x16x32_bf16 v[68:71], v[192:195], v[152:155], v[68:71]\n"
      "ds_read_b128 v[124:127], v234 offset:320\n"
      "v_mfma_f32_16x16x32_bf16 v[72:75], v[192:195], v[160:163], v[72:75]\n"
      "ds_read_b128 v[128:131], v234 offset:512\n"
      "v_mfma_f32_16x16x32_bf16 v[72:75], v[196:199], v[164:167], v[72:75]\n"
      "ds_read_b128 v[132:135], v234 offset:576\n"
      "v_mfma_f32_16x16x32_bf16 v[76:79], v[196:199], v[172:175], v[76:79]\n"
      "v_mfma_f32_16x16x32_bf16 v[76:79], v[192:195], v[168:171], v[76:79]\n"
      "v_mfma_f32_16x16x32_bf16 v[80:83], v[200:203], v[144:147], v[80:83]\n"
      "v_mfma_f32_16x16x32_bf16 v[80:83], v[204:207], v[148:151], v[80:83]\n"
      "v_mfma_f32_16x16x32_bf16 v[84:87], v[204:207], v[156:159], v[84:87]\n"
      "v_mfma_f32_16x16x32_bf16 v[84:87], v[200:203], v[152:155], v[84:87]\n"
      "v_mfma_f32_16x16x32_bf16 v[88:91], v[200:203], v[160:163], v[88:91]\n"
      "v_mfma_f32_16x16x32_bf16 v[88:91], v[204:207], v[164:167], v[88:91]\n"
      "v_mfma_f32_16x16x32_bf16 v[92:95], v[204:207], v[172:175], v[92:95]\n"
      "v_mfma_f32_16x16x32_bf16 v[92:95], v[200:203], v[168:171], v[92:95]\n"
      "s_waitcnt lgkmcnt(0)\n"
      "s_setprio 0\n"
      "v_pk_fma_f32 v[0:1], v[216:217], v[112:113], v[0:1] op_sel_hi:[0,1,1]\n"
      "v_pk_fma_f32 v[2:3], v[216:217], v[114:115], v[2:3] op_sel_hi:[0,1,1]\n"
      "v_pk_fma_f32 v[16:17], v[216:217], v[116:117], v[16:17] op_sel_hi:[0,1,1]\n"
      "v_pk_fma_f32 v[18:19], v[216:217], v[118:119], v[18:19] op_sel_hi:[0,1,1]\n"
      "v_pk_mul_f32 v[144:145], v[216:217], v[0:1] op_sel:[1,0] op_sel_hi:[1,1]\n"
      "v_pk_mul_f32 v[146:147], v[216:217], v[2:3] op_sel:[1,0] op_sel_hi:[1,1]\n"
      "v_pk_mul_f32 v[148:149], v[216:217], v[16:17] op_sel:[1,0] op_sel_hi:[1,1]\n"
      "v_pk_mul_f32 v[150:151], v[216:217], v[18:19] op_sel:[1,0] op_sel_hi:[1,1]\n"
      "v_exp_f32 v144, v144\n"
      "v_exp_f32 v145, v145\n"
      "v_exp_f32 v146, v146\n"
      "v_exp_f32 v147, v147\n"
      "v_exp_f32 v148, v148\n"
      "v_exp_f32 v149, v149\n"
      "v_exp_f32 v150, v150\n"
      "v_exp_f32 v151, v151\n"
      "v_pk_fma_f32 v[144:145], v[144:145], v[218:219], v[218:219] op_sel_hi:[1,0,0]\n"
      "v_pk_fma_f32 v[146:147], v[146:147], v[218:219], v[218:219] op_sel_hi:[1,0,0]\n"
      "v_pk_fma_f32 v[148:149], v[148:149], v[218:219], v[218:219] op_sel_hi:[1,0,0]\n"
      "v_pk_fma_f32 v[150:151], v[150:151], v[218:219], v[218:219] op_sel_hi:[1,0,0]\n"
      "v_rcp_f32 v144, v144\n"
      "v_rcp_f32 v145, v145\n"
      "v_rcp_f32 v146, v146\n"
      "v_rcp_f32 v147, v147\n"
      "v_rcp_f32 v148, v148\n"
      "v_rcp_f32 v149, v149\n"
      "v_rcp_f32 v150, v150\n"
      "v_rcp_f32 v151, v151\n"
      "v_pk_mul_f32 v[0:1], v[0:1], v[144:145]\n"
      "v_pk_mul_f32 v[2:3], v[2:3], v[146:147]\n"
      "v_pk_mul_f32 v[16:17], v[16:17], v[148:149]\n"
      "v_pk_mul_f32 v[18:19], v[18:19], v[150:151]\n"
      "v_cvt_pk_bf16_f32 v152, v0, v1\n"
      "v_cvt_pk_bf16_f32 v153, v2, v3\n"
      "v_cvt_pk_bf16_f32 v154, v16, v17\n"
      "v_cvt_pk_bf16_f32 v155, v18, v19\n"
      "buffer_store_dwordx4 v[152:155], v253, s[32:35], s67 offen sc1\n"
      "v_pk_fma_f32 v[4:5], v[220:221], v[112:113], v[4:5] op_sel_hi:[0,1,1]\n"
      "v_pk_fma_f32 v[6:7], v[220:221], v[114:115], v[6:7] op_sel_hi:[0,1,1]\n"
      "v_pk_fma_f32 v[20:21], v[220:221], v[116:117], v[20:21] op_sel_hi:[0,1,1]\n"
      "v_pk_fma_f32 v[22:23], v[220:221], v[118:119], v[22:23] op_sel_hi:[0,1,1]\n"
      "v_pk_mul_f32 v[144:145], v[220:221], v[4:5] op_sel:[1,0] op_sel_hi:[1,1]\n"
      "v_pk_mul_f32 v[146:147], v[220:221], v[6:7] op_sel:[1,0] op_sel_hi:[1,1]\n"
      "v_pk_mul_f32 v[148:149], v[220:221], v[20:21] op_sel:[1,0] op_sel_hi:[1,1]\n"
      "v_pk_mul_f32 v[150:151], v[220:221], v[22:23] op_sel:[1,0] op_sel_hi:[1,1]\n"
      "v_exp_f32 v144, v144\n"
      "v_exp_f32 v145, v145\n"
      "v_exp_f32 v146, v146\n"
      "v_exp_f32 v147, v147\n"
      "v_exp_f32 v148, v148\n"
      "v_exp_f32 v149, v149\n"
      "v_exp_f32 v150, v150\n"
      "v_exp_f32 v151, v151\n"
      "v_pk_fma_f32 v[144:145], v[144:145], v[222:223], v[222:223] op_sel_hi:[1,0,0]\n"
      "v_pk_fma_f32 v[146:147], v[146:147], v[222:223], v[222:223] op_sel_hi:[1,0,0]\n"
      "v_pk_fma_f32 v[148:149], v[148:149], v[222:223], v[222:223] op_sel_hi:[1,0,0]\n"
      "v_pk_fma_f32 v[150:151], v[150:151], v[222:223], v[222:223] op_sel_hi:[1,0,0]\n"
      "v_rcp_f32 v144, v144\n"
      "v_rcp_f32 v145, v145\n"
      "v_rcp_f32 v146, v146\n"
      "v_rcp_f32 v147, v147\n"
      "v_rcp_f32 v148, v148\n"
      "v_rcp_f32 v149, v149\n"
      "v_rcp_f32 v150, v150\n"
      "v_rcp_f32 v151, v151\n"
      "v_pk_mul_f32 v[4:5], v[4:5], v[144:145]\n"
      "v_pk_mul_f32 v[6:7], v[6:7], v[146:147]\n"
      "v_pk_mul_f32 v[20:21], v[20:21], v[148:149]\n"
      "v_pk_mul_f32 v[22:23], v[22:23], v[150:151]\n"
      "v_cvt_pk_bf16_f32 v156, v4, v5\n"
      "v_cvt_pk_bf16_f32 v157, v6, v7\n"
      "v_cvt_pk_bf16_f32 v158, v20, v21\n"
      "v_cvt_pk_bf16_f32 v159, v22, v23\n"
      "buffer_store_dwordx4 v[156:159], v253, s[32:35], s67 offen offset:1024 sc1\n"
      "v_pk_fma_f32 v[8:9], v[224:225], v[112:113], v[8:9] op_sel_hi:[0,1,1]\n"
      "v_pk_fma_f32 v[10:11], v[224:225], v[114:115], v[10:11] op_sel_hi:[0,1,1]\n"
      "v_pk_fma_f32 v[24:25], v[224:225], v[116:117], v[24:25] op_sel_hi:[0,1,1]\n"
      "v_pk_fma_f32 v[26:27], v[224:225], v[118:119], v[26:27] op_sel_hi:[0,1,1]\n"
      "v_pk_mul_f32 v[144:145], v[224:225], v[8:9] op_sel:[1,0] op_sel_hi:[1,1]\n"
      "v_pk_mul_f32 v[146:147], v[224:225], v[10:11] op_sel:[1,0] op_sel_hi:[1,1]\n"
      "v_pk_mul_f32 v[148:149], v[224:225], v[24:25] op_sel:[1,0] op_sel_hi:[1,1]\n"
      "v_pk_mul_f32 v[150:151], v[224:225], v[26:27] op_sel:[1,0] op_sel_hi:[1,1]\n"
      "v_exp_f32 v144, v144\n"
      "v_exp_f32 v145, v145\n"
      "v_exp_f32 v146, v146\n"
      "v_exp_f32 v147, v147\n"
      "v_exp_f32 v148, v148\n"
      "v_exp_f32 v149, v149\n"
      "v_exp_f32 v150, v150\n"
      "v_exp_f32 v151, v151\n"
      "v_pk_fma_f32 v[144:145], v[144:145], v[226:227], v[226:227] op_sel_hi:[1,0,0]\n"
      "v_pk_fma_f32 v[146:147], v[146:147], v[226:227], v[226:227] op_sel_hi:[1,0,0]\n"
      "v_pk_fma_f32 v[148:149], v[148:149], v[226:227], v[226:227] op_sel_hi:[1,0,0]\n"
      "v_pk_fma_f32 v[150:151], v[150:151], v[226:227], v[226:227] op_sel_hi:[1,0,0]\n"
      "v_rcp_f32 v144, v144\n"
      "v_rcp_f32 v145, v145\n"
      "v_rcp_f32 v146, v146\n"
      "v_rcp_f32 v147, v147\n"
      "v_rcp_f32 v148, v148\n"
      "v_rcp_f32 v149, v149\n"
      "v_rcp_f32 v150, v150\n"
      "v_rcp_f32 v151, v151\n"
      "v_pk_mul_f32 v[8:9], v[8:9], v[144:145]\n"
      "v_pk_mul_f32 v[10:11], v[10:11], v[146:147]\n"
      "v_pk_mul_f32 v[24:25], v[24:25], v[148:149]\n"
      "v_pk_mul_f32 v[26:27], v[26:27], v[150:151]\n"
      "v_cvt_pk_bf16_f32 v152, v8, v9\n"
      "v_cvt_pk_bf16_f32 v153, v10, v11\n"
      "v_cvt_pk_bf16_f32 v154, v24, v25\n"
      "v_cvt_pk_bf16_f32 v155, v26, v27\n"
      "buffer_store_dwordx4 v[152:155], v253, s[32:35], s70 offen sc1\n"
      "v_pk_fma_f32 v[12:13], v[228:229], v[112:113], v[12:13] op_sel_hi:[0,1,1]\n"
      "v_pk_fma_f32 v[14:15], v[228:229], v[114:115], v[14:15] op_sel_hi:[0,1,1]\n"
      "v_pk_fma_f32 v[28:29], v[228:229], v[116:117], v[28:29] op_sel_hi:[0,1,1]\n"
      "v_pk_fma_f32 v[30:31], v[228:229], v[118:119], v[30:31] op_sel_hi:[0,1,1]\n"
      "v_pk_mul_f32 v[144:145], v[228:229], v[12:13] op_sel:[1,0] op_sel_hi:[1,1]\n"
      "v_pk_mul_f32 v[146:147], v[228:229], v[14:15] op_sel:[1,0] op_sel_hi:[1,1]\n"
      "v_pk_mul_f32 v[148:149], v[228:229], v[28:29] op_sel:[1,0] op_sel_hi:[1,1]\n"
      "v_pk_mul_f32 v[150:151], v[228:229], v[30:31] op_sel:[1,0] op_sel_hi:[1,1]\n"
      "v_exp_f32 v144, v144\n"
      "v_exp_f32 v145, v145\n"
      "v_exp_f32 v146, v146\n"
      "v_exp_f32 v147, v147\n"
      "v_exp_f32 v148, v148\n"
      "v_exp_f32 v149, v149\n"
      "v_exp_f32 v150, v150\n"
      "v_exp_f32 v151, v151\n"
      "v_pk_fma_f32 v[144:145], v[144:145], v[230:231], v[230:231] op_sel_hi:[1,0,0]\n"
      "v_pk_fma_f32 v[146:147], v[146:147], v[230:231], v[230:231] op_sel_hi:[1,0,0]\n"
      "v_pk_fma_f32 v[148:149], v[148:149], v[230:231], v[230:231] op_sel_hi:[1,0,0]\n"
      "v_pk_fma_f32 v[150:151], v[150:151], v[230:231], v[230:231] op_sel_hi:[1,0,0]\n"
      "v_rcp_f32 v144, v144\n"
      "v_rcp_f32 v145, v145\n"
      "v_rcp_f32 v146, v146\n"
      "v_rcp_f32 v147, v147\n"
      "v_rcp_f32 v148, v148\n"
      "v_rcp_f32 v149, v149\n"
      "v_rcp_f32 v150, v150\n"
      "v_rcp_f32 v151, v151\n"
      "v_pk_mul_f32 v[12:13], v[12:13], v[144:145]\n"
      "v_pk_mul_f32 v[14:15], v[14:15], v[146:147]\n"
      "v_pk_mul_f32 v[28:29], v[28:29], v[148:149]\n"
      "v_pk_mul_f32 v[30:31], v[30:31], v[150:151]\n"
      "v_cvt_pk_bf16_f32 v156, v12, v13\n"
      "v_cvt_pk_bf16_f32 v157, v14, v15\n"
      "v_cvt_pk_bf16_f32 v158, v28, v29\n"
      "v_cvt_pk_bf16_f32 v159, v30, v31\n"
      "buffer_store_dwordx4 v[156:159], v253, s[32:35], s70 offen offset:1024 sc1\n"
      "v_pk_fma_f32 v[32:33], v[216:217], v[120:121], v[32:33] op_sel_hi:[0,1,1]\n"
      "v_pk_fma_f32 v[34:35], v[216:217], v[122:123], v[34:35] op_sel_hi:[0,1,1]\n"
      "v_pk_fma_f32 v[48:49], v[216:217], v[124:125], v[48:49] op_sel_hi:[0,1,1]\n"
      "v_pk_fma_f32 v[50:51], v[216:217], v[126:127], v[50:51] op_sel_hi:[0,1,1]\n"
      "v_pk_mul_f32 v[144:145], v[216:217], v[32:33] op_sel:[1,0] op_sel_hi:[1,1]\n"
      "v_pk_mul_f32 v[146:147], v[216:217], v[34:35] op_sel:[1,0] op_sel_hi:[1,1]\n"
      "v_pk_mul_f32 v[148:149], v[216:217], v[48:49] op_sel:[1,0] op_sel_hi:[1,1]\n"
      "v_pk_mul_f32 v[150:151], v[216:217], v[50:51] op_sel:[1,0] op_sel_hi:[1,1]\n"
      "v_exp_f32 v144, v144\n"
      "v_exp_f32 v145, v145\n"
      "v_exp_f32 v146, v146\n"
      "v_exp_f32 v147, v147\n"
      "v_exp_f32 v148, v148\n"
      "v_exp_f32 v149, v149\n"
      "v_exp_f32 v150, v150\n"
      "v_exp_f32 v151, v151\n"
      "v_pk_fma_f32 v[144:145], v[144:145], v[218:219], v[218:219] op_sel_hi:[1,0,0]\n"
      "v_pk_fma_f32 v[146:147], v[146:147], v[218:219], v[218:219] op_sel_hi:[1,0,0]\n"
      "v_pk_fma_f32 v[148:149], v[148:149], v[218:219], v[218:219] op_sel_hi:[1,0,0]\n"
      "v_pk_fma_f32 v[150:151], v[150:151], v[218:219], v[218:219] op_sel_hi:[1,0,0]\n"
      "v_rcp_f32 v144, v144\n"
      "v_rcp_f32 v145, v145\n"
      "v_rcp_f32 v146, v146\n"
      "v_rcp_f32 v147, v147\n"
      "v_rcp_f32 v148, v148\n"
      "v_rcp_f32 v149, v149\n"
      "v_rcp_f32 v150, v150\n"
      "v_rcp_f32 v151, v151\n"
      "v_pk_mul_f32 v[32:33], v[32:33], v[144:145]\n"
      "v_pk_mul_f32 v[34:35], v[34:35], v[146:147]\n"
      "v_pk_mul_f32 v[48:49], v[48:49], v[148:149]\n"
      "v_pk_mul_f32 v[50:51], v[50:51], v[150:151]\n"
      "v_cvt_pk_bf16_f32 v152, v32, v33\n"
      "v_cvt_pk_bf16_f32 v153, v34, v35\n"
      "v_cvt_pk_bf16_f32 v154, v48, v49\n"
      "v_cvt_pk_bf16_f32 v155, v50, v51\n"
      "buffer_store_dwordx4 v[152:155], v253, s[32:35], s68 offen sc1\n"
      "v_pk_fma_f32 v[36:37], v[220:221], v[120:121], v[36:37] op_sel_hi:[0,1,1]\n"
      "v_pk_fma_f32 v[38:39], v[220:221], v[122:123], v[38:39] op_sel_hi:[0,1,1]\n"
      "v_pk_fma_f32 v[52:53], v[220:221], v[124:125], v[52:53] op_sel_hi:[0,1,1]\n"
      "v_pk_fma_f32 v[54:55], v[220:221], v[126:127], v[54:55] op_sel_hi:[0,1,1]\n"
      "v_pk_mul_f32 v[144:145], v[220:221], v[36:37] op_sel:[1,0] op_sel_hi:[1,1]\n"
      "v_pk_mul_f32 v[146:147], v[220:221], v[38:39] op_sel:[1,0] op_sel_hi:[1,1]\n"
      "v_pk_mul_f32 v[148:149], v[220:221], v[52:53] op_sel:[1,0] op_sel_hi:[1,1]\n"
      "v_pk_mul_f32 v[150:151], v[220:221], v[54:55] op_sel:[1,0] op_sel_hi:[1,1]\n"
      "v_exp_f32 v144, v144\n"
      "v_exp_f32 v145, v145\n"
      "v_exp_f32 v146, v146\n"
      "v_exp_f32 v147, v147\n"
      "v_exp_f32 v148, v148\n"
      "v_exp_f32 v149, v149\n"
      "v_exp_f32 v150, v150\n"
      "v_exp_f32 v151, v151\n"
      "v_pk_fma_f32 v[144:145], v[144:145], v[222:223], v[222:223] op_sel_hi:[1,0,0]\n"
      "v_pk_fma_f32 v[146:147], v[146:147], v[222:223], v[222:223] op_sel_hi:[1,0,0]\n"
      "v_pk_fma_f32 v[148:149], v[148:149], v[222:223], v[222:223] op_sel_hi:[1,0,0]\n"
      "v_pk_fma_f32 v[150:151], v[150:151], v[222:223], v[222:223] op_sel_hi:[1,0,0]\n"
      "v_rcp_f32 v144, v144\n"
      "v_rcp_f32 v145, v145\n"
      "v_rcp_f32 v146, v146\n"
      "v_rcp_f32 v147, v147\n"
      "v_rcp_f32 v148, v148\n"
      "v_rcp_f32 v149, v149\n"
      "v_rcp_f32 v150, v150\n"
      "v_rcp_f32 v151, v151\n"
      "v_pk_mul_f32 v[36:37], v[36:37], v[144:145]\n"
      "v_pk_mul_f32 v[38:39], v[38:39], v[146:147]\n"
      "v_pk_mul_f32 v[52:53], v[52:53], v[148:149]\n"
      "v_pk_mul_f32 v[54:55], v[54:55], v[150:151]\n"
      "v_cvt_pk_bf16_f32 v156, v36, v37\n"
      "v_cvt_pk_bf16_f32 v157, v38, v39\n"
      "v_cvt_pk_bf16_f32 v158, v52, v53\n"
      "v_cvt_pk_bf16_f32 v159, v54, v55\n"
      "buffer_store_dwordx4 v[156:159], v253, s[32:35], s68 offen offset:1024 sc1\n"
      "v_pk_fma_f32 v[40:41], v[224:225], v[120:121], v[40:41] op_sel_hi:[0,1,1]\n"
      "v_pk_fma_f32 v[42:43], v[224:225], v[122:123], v[42:43] op_sel_hi:[0,1,1]\n"
      "v_pk_fma_f32 v[56:57], v[224:225], v[124:125], v[56:57] op_sel_hi:[0,1,1]\n"
      "v_pk_fma_f32 v[58:59], v[224:225], v[126:127], v[58:59] op_sel_hi:[0,1,1]\n"
      "v_pk_mul_f32 v[144:145], v[224:225], v[40:41] op_sel:[1,0] op_sel_hi:[1,1]\n"
      "v_pk_mul_f32 v[146:147], v[224:225], v[42:43] op_sel:[1,0] op_sel_hi:[1,1]\n"
      "v_pk_mul_f32 v[148:149], v[224:225], v[56:57] op_sel:[1,0] op_sel_hi:[1,1]\n"
      "v_pk_mul_f32 v[150:151], v[224:225], v[58:59] op_sel:[1,0] op_sel_hi:[1,1]\n"
      "v_exp_f32 v144, v144\n"
      "v_exp_f32 v145, v145\n"
      "v_exp_f32 v146, v146\n"
      "v_exp_f32 v147, v147\n"
      "v_exp_f32 v148, v148\n"
      "v_exp_f32 v149, v149\n"
      "v_exp_f32 v150, v150\n"
      "v_exp_f32 v151, v151\n"
      "v_pk_fma_f32 v[144:145], v[144:145], v[226:227], v[226:227] op_sel_hi:[1,0,0]\n"
      "v_pk_fma_f32 v[146:147], v[146:147], v[226:227], v[226:227] op_sel_hi:[1,0,0]\n"
      "v_pk_fma_f32 v[148:149], v[148:149], v[226:227], v[226:227] op_sel_hi:[1,0,0]\n"
      "v_pk_fma_f32 v[150:151], v[150:151], v[226:227], v[226:227] op_sel_hi:[1,0,0]\n"
      "v_rcp_f32 v144, v144\n"
      "v_rcp_f32 v145, v145\n"
      "v_rcp_f32 v146, v146\n"
      "v_rcp_f32 v147, v147\n"
      "v_rcp_f32 v148, v148\n"
      "v_rcp_f32 v149, v149\n"
      "v_rcp_f32 v150, v150\n"
      "v_rcp_f32 v151, v151\n"
      "v_pk_mul_f32 v[40:41], v[40:41], v[144:145]\n"
      "v_pk_mul_f32 v[42:43], v[42:43], v[146:147]\n"
      "v_pk_mul_f32 v[56:57], v[56:57], v[148:149]\n"
      "v_pk_mul_f32 v[58:59], v[58:59], v[150:151]\n"
      "v_cvt_pk_bf16_f32 v152, v40, v41\n"
      "v_cvt_pk_bf16_f32 v153, v42, v43\n"
      "v_cvt_pk_bf16_f32 v154, v56, v57\n"
      "v_cvt_pk_bf16_f32 v155, v58, v59\n"
      "buffer_store_dwordx4 v[152:155], v253, s[32:35], s71 offen sc1\n"
      "v_pk_fma_f32 v[44:45], v[228:229], v[120:121], v[44:45] op_sel_hi:[0,1,1]\n"
      "v_pk_fma_f32 v[46:47], v[228:229], v[122:123], v[46:47] op_sel_hi:[0,1,1]\n"
      "v_pk_fma_f32 v[60:61], v[228:229], v[124:125], v[60:61] op_sel_hi:[0,1,1]\n"
      "v_pk_fma_f32 v[62:63], v[228:229], v[126:127], v[62:63] op_sel_hi:[0,1,1]\n"
      "v_pk_mul_f32 v[144:145], v[228:229], v[44:45] op_sel:[1,0] op_sel_hi:[1,1]\n"
      "v_pk_mul_f32 v[146:147], v[228:229], v[46:47] op_sel:[1,0] op_sel_hi:[1,1]\n"
      "v_pk_mul_f32 v[148:149], v[228:229], v[60:61] op_sel:[1,0] op_sel_hi:[1,1]\n"
      "v_pk_mul_f32 v[150:151], v[228:229], v[62:63] op_sel:[1,0] op_sel_hi:[1,1]\n"
      "v_exp_f32 v144, v144\n"
      "v_exp_f32 v145, v145\n"
      "v_exp_f32 v146, v146\n"
      "v_exp_f32 v147, v147\n"
      "v_exp_f32 v148, v148\n"
      "v_exp_f32 v149, v149\n"
      "v_exp_f32 v150, v150\n"
      "v_exp_f32 v151, v151\n"
      "v_pk_fma_f32 v[144:145], v[144:145], v[230:231], v[230:231] op_sel_hi:[1,0,0]\n"
      "v_pk_fma_f32 v[146:147], v[146:147], v[230:231], v[230:231] op_sel_hi:[1,0,0]\n"
      "v_pk_fma_f32 v[148:149], v[148:149], v[230:231], v[230:231] op_sel_hi:[1,0,0]\n"
      "v_pk_fma_f32 v[150:151], v[150:151], v[230:231], v[230:231] op_sel_hi:[1,0,0]\n"
      "ds_write_b32 v240, v240\n"
      "v_rcp_f32 v144, v144\n"
      "v_rcp_f32 v145, v145\n"
      "v_rcp_f32 v146, v146\n"
      "v_rcp_f32 v147, v147\n"
      "v_rcp_f32 v148, v148\n"
      "v_rcp_f32 v149, v149\n"
      "v_rcp_f32 v150, v150\n"
      "v_rcp_f32 v151, v151\n"
      "v_mul_f32 v44, v44, v144\n"
      "v_mul_f32 v45, v45, v145\n"
      "v_mul_f32 v46, v46, v146\n"
      "v_mul_f32 v47, v47, v147\n"
      "v_mul_f32 v60, v60, v148\n"
      "v_mul_f32 v61, v61, v149\n"
      "v_mul_f32 v62, v62, v150\n"
      "v_mul_f32 v63, v63, v151\n"
      "v_cvt_pk_bf16_f32 v156, v44, v45\n"
      "v_cvt_pk_bf16_f32 v157, v46, v47\n"
      "v_cvt_pk_bf16_f32 v158, v60, v61\n"
      "v_cvt_pk_bf16_f32 v159, v62, v63\n"
      "buffer_store_dwordx4 v[156:159], v253, s[32:35], s71 offen offset:1024 sc1\n"
      "s_waitcnt vmcnt(4)\n"
      "s_mov_b64 exec, 1\n"
      "v_mov_b32 v190, 160748\n"
      "v_mov_b32 v191, 1\n"
      "ds_add_rtn_u32 v191, v190, v191\n"
      "s_waitcnt lgkmcnt(0)\n"
      "v_readfirstlane_b32 s50, v191\n"
      "s_nop 0\n"
      "s_cmp_eq_u32 s50, 3\n"
      "s_cselect_b32 s50, s59, 0x7fff0000\n"
      "s_nop 1\n"
      "buffer_store_dwordx2 v[248:249], off, s[32:35], s50 sc1\n"
      "s_mov_b64 exec, -1\n"
      "buffer_load_dwordx4 v[24:27], v236, s[40:43], s83 offen\n"
      "buffer_load_dwordx4 v[24:27], v236, s[40:43], s61 offen offset:0\n"
      "v_fmac_f32 v96, v216, v136\n"
      "v_fmac_f32 v97, v216, v137\n"
      "v_fmac_f32 v98, v216, v138\n"
      "v_fmac_f32 v99, v216, v139\n"
      "v_fmac_f32 v104, v216, v140\n"
      "v_fmac_f32 v105, v216, v141\n"
      "v_fmac_f32 v106, v216, v142\n"
      "v_fmac_f32 v107, v216, v143\n"
      "v_mul_f32 v144, v217, v96\n"
      "v_mul_f32 v145, v217, v97\n"
      "v_mul_f32 v146, v217, v98\n"
      "v_mul_f32 v147, v217, v99\n"
      "v_mul_f32 v148, v217, v104\n"
      "v_mul_f32 v149, v217, v105\n"
      "v_mul_f32 v150, v217, v106\n"
      "v_mul_f32 v151, v217, v107\n"
      "v_exp_f32 v144, v144\n"
      "v_exp_f32 v145, v145\n"
      "v_exp_f32 v146, v146\n"
      "v_exp_f32 v147, v147\n"
      "v_exp_f32 v148, v148\n"
      "v_exp_f32 v149, v149\n"
      "v_exp_f32 v150, v150\n"
      "v_exp_f32 v151, v151\n"
      "buffer_load_dwordx4 v[24:27], v237, s[40:43], s61 offen offset:1032\n"
      "v_fma_f32 v144, v144, v218, v218\n"
      "v_fma_f32 v145, v145, v218, v218\n"
      "v_fma_f32 v146, v146, v218, v218\n"
      "v_fma_f32 v147, v147, v218, v218\n"
      "v_fma_f32 v148, v148, v218, v218\n"
      "v_fma_f32 v149, v149, v218, v218\n"
      "v_fma_f32 v150, v150, v218, v218\n"
      "v_fma_f32 v151, v151, v218, v218\n"
      "v_rcp_f32 v144, v144\n"
      "v_rcp_f32 v145, v145\n"
      "v_rcp_f32 v146, v146\n"
      "v_rcp_f32 v147, v147\n"
      "v_rcp_f32 v148, v148\n"
      "v_rcp_f32 v149, v149\n"
      "v_rcp_f32 v150, v150\n"
      "v_rcp_f32 v151, v151\n"
      "v_mul_f32 v96, v96, v144\n"
      "v_mul_f32 v97, v97, v145\n"
      "v_mul_f32 v98, v98, v146\n"
      "v_mul_f32 v99, v99, v147\n"
      "v_mul_f32 v104, v104, v148\n"
      "v_mul_f32 v105, v105, v149\n"
      "v_mul_f32 v106, v106, v150\n"
      "v_mul_f32 v107, v107, v151\n"
      "v_cvt_pk_bf16_f32 v152, v96, v97\n"
      "v_cvt_pk_bf16_f32 v153, v98, v99\n"
      "v_cvt_pk_bf16_f32 v154, v104, v105\n"
      "v_cvt_pk_bf16_f32 v155, v106, v107\n"
      "buffer_store_dwordx4 v[152:155], v253, s[32:35], s66 offen sc1\n"
      "buffer_load_dwordx4 v[24:27], v238, s[40:43], s61 offen offset:2064\n"
      "v_fmac_f32 v100, v220, v136\n"
      "v_fmac_f32 v101, v220, v137\n"
      "v_fmac_f32 v102, v220, v138\n"
      "v_fmac_f32 v103, v220, v139\n"
      "v_fmac_f32 v108, v220, v140\n"
      "v_fmac_f32 v109, v220, v141\n"
      "v_fmac_f32 v110, v220, v142\n"
      "v_fmac_f32 v111, v220, v143\n"
      "v_mul_f32 v144, v221, v100\n"
      "v_mul_f32 v145, v221, v101\n"
      "v_mul_f32 v146, v221, v102\n"
      "v_mul_f32 v147, v221, v103\n"
      "v_mul_f32 v148, v221, v108\n"
      "v_mul_f32 v149, v221, v109\n"
      "v_mul_f32 v150, v221, v110\n"
      "v_mul_f32 v151, v221, v111\n"
      "v_exp_f32 v144, v144\n"
      "v_exp_f32 v145, v145\n"
      "v_exp_f32 v146, v146\n"
      "v_exp_f32 v147, v147\n"
      "v_exp_f32 v148, v148\n"
      "v_exp_f32 v149, v149\n"
      "v_exp_f32 v150, v150\n"
      "v_exp_f32 v151, v151\n"
      "buffer_load_dwordx4 v[24:27], v239, s[40:43], s61 offen offset:3096\n"
      "v_fma_f32 v144, v144, v222, v222\n"
      "v_fma_f32 v145, v145, v222, v222\n"
      "v_fma_f32 v146, v146, v222, v222\n"
      "v_fma_f32 v147, v147, v222, v222\n"
      "v_fma_f32 v148, v148, v222, v222\n"
      "v_fma_f32 v149, v149, v222, v222\n"
      "v_fma_f32 v150, v150, v222, v222\n"
      "v_fma_f32 v151, v151, v222, v222\n"
      "v_rcp_f32 v144, v144\n"
      "v_rcp_f32 v145, v145\n"
      "v_rcp_f32 v146, v146\n"
      "v_rcp_f32 v147, v147\n"
      "v_rcp_f32 v148, v148\n"
      "v_rcp_f32 v149, v149\n"
      "v_rcp_f32 v150, v150\n"
      "v_rcp_f32 v151, v151\n"
      "v_mul_f32 v100, v100, v144\n"
      "v_mul_f32 v101, v101, v145\n"
      "v_mul_f32 v102, v102, v146\n"
      "v_mul_f32 v103, v103, v147\n"
      "v_mul_f32 v108, v108, v148\n"
      "v_mul_f32 v109, v109, v149\n"
      "v_mul_f32 v110, v110, v150\n"
      "v_mul_f32 v111, v111, v151\n"
      "v_cvt_pk_bf16_f32 v156, v100, v101\n"
      "v_cvt_pk_bf16_f32 v157, v102, v103\n"
      "v_cvt_pk_bf16_f32 v158, v108, v109\n"
      "v_cvt_pk_bf16_f32 v159, v110, v111\n"
      "buffer_store_dwordx4 v[156:159], v253, s[32:35], s66 offen offset:1024 sc1\n"
      "s_add_u32 m0, s77, 32896\n"
      "s_nop 0\n"
      "buffer_load_dwordx4 v236, s[40:43], s63 offen lds\n"
      "v_fmac_f32 v64, v216, v128\n"
      "v_fmac_f32 v65, v216, v129\n"
      "v_fmac_f32 v66, v216, v130\n"
      "v_fmac_f32 v67, v216, v131\n"
      "v_fmac_f32 v80, v216, v132\n"
      "v_fmac_f32 v81, v216, v133\n"
      "v_fmac_f32 v82, v216, v134\n"
      "v_fmac_f32 v83, v216, v135\n"
      "v_mul_f32 v144, v217, v64\n"
      "v_mul_f32 v145, v217, v65\n"
      "v_mul_f32 v146, v217, v66\n"
      "v_mul_f32 v147, v217, v67\n"
      "v_mul_f32 v148, v217, v80\n"
      "v_mul_f32 v149, v217, v81\n"
      "v_mul_f32 v150, v217, v82\n"
      "v_mul_f32 v151, v217, v83\n"
      "v_exp_f32 v144, v144\n"
      "v_exp_f32 v145, v145\n"
      "v_exp_f32 v146, v146\n"
      "v_exp_f32 v147, v147\n"
      "v_exp_f32 v148, v148\n"
      "v_exp_f32 v149, v149\n"
      "v_exp_f32 v150, v150\n"
      "v_exp_f32 v151, v151\n"
      "s_add_u32 m0, s77, 32896\n"
      "s_nop 0\n"
      "buffer_load_dwordx4 v237, s[40:43], s63 offen offset:1032 lds\n"
      "v_fma_f32 v144, v144, v218, v218\n"
      "v_fma_f32 v145, v145, v218, v218\n"
      "v_fma_f32 v146, v146, v218, v218\n"
      "v_fma_f32 v147, v147, v218, v218\n"
      "v_fma_f32 v148, v148, v218, v218\n"
      "v_fma_f32 v149, v149, v218, v218\n"
      "v_fma_f32 v150, v150, v218, v218\n"
      "v_fma_f32 v151, v151, v218, v218\n"
      "v_rcp_f32 v144, v144\n"
      "v_rcp_f32 v145, v145\n"
      "v_rcp_f32 v146, v146\n"
      "v_rcp_f32 v147, v147\n"
      "v_rcp_f32 v148, v148\n"
      "v_rcp_f32 v149, v149\n"
      "v_rcp_f32 v150, v150\n"
      "v_rcp_f32 v151, v151\n"
      "v_mul_f32 v64, v64, v144\n"
      "v_mul_f32 v65, v65, v145\n"
      "v_mul_f32 v66, v66, v146\n"
      "v_mul_f32 v67, v67, v147\n"
      "v_mul_f32 v80, v80, v148\n"
      "v_mul_f32 v81, v81, v149\n"
      "v_mul_f32 v82, v82, v150\n"
      "v_mul_f32 v83, v83, v151\n"
      "v_cvt_pk_bf16_f32 v152, v64, v65\n"
      "v_cvt_pk_bf16_f32 v153, v66, v67\n"
      "v_cvt_pk_bf16_f32 v154, v80, v81\n"
      "v_cvt_pk_bf16_f32 v155, v82, v83\n"
      "buffer_store_dwordx4 v[152:155], v253, s[32:35], s69 offen sc1\n"
      "s_add_u32 m0, s77, 32896\n"
      "s_nop 0\n"
      "buffer_load_dwordx4 v238, s[40:43], s63 offen offset:2064 lds\n"
      "v_fmac_f32 v68, v220, v128\n"
      "v_fmac_f32 v69, v220, v129\n"
      "v_fmac_f32 v70, v220, v130\n"
      "v_fmac_f32 v71, v220, v131\n"
      "v_fmac_f32 v84, v220, v132\n"
      "v_fmac_f32 v85, v220, v133\n"
      "v_fmac_f32 v86, v220, v134\n"
      "v_fmac_f32 v87, v220, v135\n"
      "v_mul_f32 v144, v221, v68\n"
      "v_mul_f32 v145, v221, v69\n"
      "v_mul_f32 v146, v221, v70\n"
      "v_mul_f32 v147, v221, v71\n"
      "v_mul_f32 v148, v221, v84\n"
      "v_mul_f32 v149, v221, v85\n"
      "v_mul_f32 v150, v221, v86\n"
      "v_mul_f32 v151, v221, v87\n"
      "v_exp_f32 v144, v144\n"
      "v_exp_f32 v145, v145\n"
      "v_exp_f32 v146, v146\n"
      "v_exp_f32 v147, v147\n"
      "v_exp_f32 v148, v148\n"
      "v_exp_f32 v149, v149\n"
      "v_exp_f32 v150, v150\n"
      "v_exp_f32 v151, v151\n"
      "s_add_u32 m0, s77, 32896\n"
      "s_nop 0\n"
      "buffer_load_dwordx4 v239, s[40:43], s63 offen offset:3096 lds\n"
      "v_fma_f32 v144, v144, v222, v222\n"
      "v_fma_f32 v145, v145, v222, v222\n"
      "v_fma_f32 v146, v146, v222, v222\n"
      "v_fma_f32 v147, v147, v222, v222\n"
      "v_fma_f32 v148, v148, v222, v222\n"
      "v_fma_f32 v149, v149, v222, v222\n"
      "v_fma_f32 v150, v150, v222, v222\n"
      "v_fma_f32 v151, v151, v222, v222\n"
      "v_rcp_f32 v144, v144\n"
      "v_rcp_f32 v145, v145\n"
      "v_rcp_f32 v146, v146\n"
      "v_rcp_f32 v147, v147\n"
      "v_rcp_f32 v148, v148\n"
      "v_rcp_f32 v149, v149\n"
      "v_rcp_f32 v150, v150\n"
      "v_rcp_f32 v151, v151\n"
      "v_mul_f32 v68, v68, v144\n"
      "v_mul_f32 v69, v69, v145\n"
      "v_mul_f32 v70, v70, v146\n"
      "v_mul_f32 v71, v71, v147\n"
      "v_mul_f32 v84, v84, v148\n"
      "v_mul_f32 v85, v85, v149\n"
      "v_mul_f32 v86, v86, v150\n"
      "v_mul_f32 v87, v87, v151\n"
      "v_cvt_pk_bf16_f32 v156, v68, v69\n"
      "v_cvt_pk_bf16_f32 v157, v70, v71\n"
      "v_cvt_pk_bf16_f32 v158, v84, v85\n"
      "v_cvt_pk_bf16_f32 v159, v86, v87\n"
      "buffer_store_dwordx4 v[156:159], v253, s[32:35], s69 offen offset:1024 sc1\n"
      "s_mov_b32 m0, 160320\n"
      "s_mov_b64 exec, 15\n"
      "buffer_load_dwordx4 v253, s[32:35], s58 offen sc1 lds\n"
      "s_mov_b64 exec, -1\n"
      "s_add_u32 m0, s77, 65792\n"
      "s_nop 0\n"
      "buffer_load_dwordx4 v236, s[40:43], s64 offen lds\n"
      "v_fmac_f32 v72, v224, v128\n"
      "v_fmac_f32 v73, v224, v129\n"
      "v_fmac_f32 v74, v224, v130\n"
      "v_fmac_f32 v75, v224, v131\n"
      "v_fmac_f32 v88, v224, v132\n"
      "v_fmac_f32 v89, v224, v133\n"
      "v_fmac_f32 v90, v224, v134\n"
      "v_fmac_f32 v91, v224, v135\n"
      "v_mul_f32 v144, v225, v72\n"
      "v_mul_f32 v145, v225, v73\n"
      "v_mul_f32 v146, v225, v74\n"
      "v_mul_f32 v147, v225, v75\n"
      "v_mul_f32 v148, v225, v88\n"
      "v_mul_f32 v149, v225, v89\n"
      "v_mul_f32 v150, v225, v90\n"
      "v_mul_f32 v151, v225, v91\n"
      "v_exp_f32 v144, v144\n"
      "v_exp_f32 v145, v145\n"
      "v_exp_f32 v146, v146\n"
      "v_exp_f32 v147, v147\n"
      "v_exp_f32 v148, v148\n"
      "v_exp_f32 v149, v149\n"
      "v_exp_f32 v150, v150\n"
      "v_exp_f32 v151, v151\n"
      "s_add_u32 m0, s77, 65792\n"
      "s_nop 0\n"
      "buffer_load_dwordx4 v237, s[40:43], s64 offen offset:1032 lds\n"
      "v_fma_f32 v144, v144, v226, v226\n"
      "v_fma_f32 v145, v145, v226, v226\n"
      "v_fma_f32 v146, v146, v226, v226\n"
      "v_fma_f32 v147, v147, v226, v226\n"
      "v_fma_f32 v148, v148, v226, v226\n"
      "v_fma_f32 v149, v149, v226, v226\n"
      "v_fma_f32 v150, v150, v226, v226\n"
      "v_fma_f32 v151, v151, v226, v226\n"
      "v_rcp_f32 v144, v144\n"
      "v_rcp_f32 v145, v145\n"
      "v_rcp_f32 v146, v146\n"
      "v_rcp_f32 v147, v147\n"
      "v_rcp_f32 v148, v148\n"
      "v_rcp_f32 v149, v149\n"
      "v_rcp_f32 v150, v150\n"
      "v_rcp_f32 v151, v151\n"
      "v_mul_f32 v72, v72, v144\n"
      "v_mul_f32 v73, v73, v145\n"
      "v_mul_f32 v74, v74, v146\n"
      "v_mul_f32 v75, v75, v147\n"
      "v_mul_f32 v88, v88, v148\n"
      "v_mul_f32 v89, v89, v149\n"
      "v_mul_f32 v90, v90, v150\n"
      "v_mul_f32 v91, v91, v151\n"
      "v_cvt_pk_bf16_f32 v152, v72, v73\n"
      "v_cvt_pk_bf16_f32 v153, v74, v75\n"
      "v_cvt_pk_bf16_f32 v154, v88, v89\n"
      "v_cvt_pk_bf16_f32 v155, v90, v91\n"
      "buffer_store_dwordx4 v[152:155], v253, s[32:35], s72 offen sc1\n"
      "s_add_u32 m0, s77, 65792\n"
      "s_nop 0\n"
      "buffer_load_dwordx4 v238, s[40:43], s64 offen offset:2064 lds\n"
      "v_add_u32 v194, 160320, v244\n"
      "s_mov_b32 s56, 200000\n"
      ".Lk_h0:\n"
      "ds_read_b64 v[192:193], v194\n"
      "s_waitcnt lgkmcnt(0)\n"
      "v_cmp_ne_u64 vcc, s[48:49], v[192:193]\n"
      "s_nop 0\n"
      "s_cbranch_vccz .Lk_h1\n"
      "s_and_b32 s50, s56, 3\n"
      "s_cmp_eq_u32 s50, s29\n"
      "s_cbranch_scc0 .Lk_h0b\n"
      "s_mov_b32 m0, 160320\n"
      "s_mov_b64 exec, 15\n"
      "buffer_load_dwordx4 v253, s[32:35], s58 offen sc1 lds\n"
      "s_mov_b64 exec, -1\n"
      ".Lk_h0b:\n"
      "s_sleep 1\n"
      "s_sub_u32 s56, s56, 1\n"
      "s_cbranch_scc0 .Lk_h0\n"
      ".Lk_h1:\n"
      "s_add_u32 m0, s65, 0\n"
      "s_nop 0\n"
      "buffer_load_dwordx4 v242, s[32:35], s62 offen sc1 lds\n"
      "buffer_load_dwordx4 v242, s[32:35], s62 offen offset:1024 sc1 lds\n"
      "buffer_load_dwordx4 v242, s[32:35], s62 offen offset:2048 sc1 lds\n"
      "buffer_load_dwordx4 v242, s[32:35], s62 offen offset:3072 sc1 lds\n"
      "v_fmac_f32 v76, v228, v128\n"
      "v_fmac_f32 v77, v228, v129\n"
      "v_fmac_f32 v78, v228, v130\n"
      "v_fmac_f32 v79, v228, v131\n"
      "v_fmac_f32 v92, v228, v132\n"
      "v_fmac_f32 v93, v228, v133\n"
      "v_fmac_f32 v94, v228, v134\n"
      "v_fmac_f32 v95, v228, v135\n"
      "v_mul_f32 v144, v229, v76\n"
      "v_mul_f32 v145, v229, v77\n"
      "v_mul_f32 v146, v229, v78\n"
      "v_mul_f32 v147, v229, v79\n"
      "v_mul_f32 v148, v229, v92\n"
      "v_mul_f32 v149, v229, v93\n"
      "v_mul_f32 v150, v229, v94\n"
      "v_mul_f32 v151, v229, v95\n"
      "v_exp_f32 v144, v144\n"
      "v_exp_f32 v145, v145\n"
      "v_exp_f32 v146, v146\n"
      "v_exp_f32 v147, v147\n"
      "v_exp_f32 v148, v148\n"
      "v_exp_f32 v149, v149\n"
      "v_exp_f32 v150, v150\n"
      "v_exp_f32 v151, v151\n"
      "s_add_u32 m0, s77, 65792\n"
      "s_nop 0\n"
      "buffer_load_dwordx4 v239, s[40:43], s64 offen offset:3096 lds\n"
      "s_add_u32 m0, s65, 32896\n"
      "s_nop 0\n"
      "buffer_load_dwordx4 v242, s[32:35], s63 offen sc1 lds\n"
      "buffer_load_dwordx4 v242, s[32:35], s63 offen offset:1024 sc1 lds\n"
      "buffer_load_dwordx4 v242, s[32:35], s63 offen offset:2048 sc1 lds\n"
      "buffer_load_dwordx4 v242, s[32:35], s63 offen offset:3072 sc1 lds\n"
      "v_fma_f32 v144, v144, v230, v230\n"
      "v_fma_f32 v145, v145, v230, v230\n"
      "v_fma_f32 v146, v146, v230, v230\n"
      "v_fma_f32 v147, v147, v230, v230\n"
      "v_fma_f32 v148, v148, v230, v230\n"
      "v_fma_f32 v149, v149, v230, v230\n"
      "v_fma_f32 v150, v150, v230, v230\n"
      "v_fma_f32 v151, v151, v230, v230\n"
      "v_rcp_f32 v144, v144\n"
      "v_rcp_f32 v145, v145\n"
      "v_rcp_f32 v146, v146\n"
      "v_rcp_f32 v147, v147\n"
      "v_rcp_f32 v148, v148\n"
      "v_rcp_f32 v149, v149\n"
      "v_rcp_f32 v150, v150\n"
      "v_rcp_f32 v151, v151\n"
      "v_mul_f32 v76, v76, v144\n"
      "v_mul_f32 v77, v77, v145\n"
      "v_mul_f32 v78, v78, v146\n"
      "v_mul_f32 v79, v79, v147\n"
      "v_mul_f32 v92, v92, v148\n"
      "v_mul_f32 v93, v93, v149\n"
      "v_mul_f32 v94, v94, v150\n"
      "v_mul_f32 v95, v95, v151\n"
      "v_cvt_pk_bf16_f32 v156, v76, v77\n"
      "v_cvt_pk_bf16_f32 v157, v78, v79\n"
      "v_cvt_pk_bf16_f32 v158, v92, v93\n"
      "v_cvt_pk_bf16_f32 v159, v94, v95\n"
      "buffer_store_dwordx4 v[156:159], v253, s[32:35], s72 offen offset:1024 sc1\n"
      "s_mov_b64 exec, 7\n"
      "global_load_dword v51, v243, s[84:85]\n"
      "s_mov_b64 exec, -1\n"
      "s_lshl_b32 s50, s25, 6\n"
      "s_lshl_b32 s51, s24, 2\n"
      "s_add_u32 s50, s50, s51\n"
      "s_add_u32 s50, s50, s29\n"
      "s_lshl_b32 s57, s50, 3\n"
      "s_xor_b32 s53, s57, 4096\n"
      "s_add_u32 s57, s57, 151552\n"
      "s_add_u32 s53, s53, 151552\n"
      "s_cmp_eq_u32 s29, 0\n"
      "s_cselect_b32 s71, 64, 0x7fff0000\n"
      "s_add_u32 s59, s59, s71\n"
      "s_add_u32 s58, s58, s71\n"
      "v_readlane_b32 s60, v241, 3\n"
      "s_waitcnt vmcnt(7)\n"
      "s_barrier\n"
      "s_add_u32 m0, s65, 65792\n"
      "s_nop 0\n"
      "buffer_load_dwordx4 v242, s[32:35], s64 offen sc1 lds\n"
      "buffer_load_dwordx4 v242, s[32:35], s64 offen offset:1024 sc1 lds\n"
      "buffer_load_dwordx4 v242, s[32:35], s64 offen offset:2048 sc1 lds\n"
      "buffer_load_dwordx4 v242, s[32:35], s64 offen offset:3072 sc1 lds\n"
      "s_add_u32 m0, s77, 0\n"
      "s_waitcnt vmcnt(5)\n"
      "s_barrier\n"
      "s_mov_b64 exec, 1\n"
      "buffer_store_dwordx2 v[248:249], off, s[32:35], s59 sc1\n"
      "s_mov_b64 exec, -1\n"
      "buffer_load_dwordx4 v236, s[40:43], s60 offen lds\n"
      "v_readlane_b32 s61, v241, 4\n"
      "buffer_load_dwordx4 v237, s[40:43], s60 offen offset:1032 lds\n"
      "buffer_load_dwordx4 v238, s[40:43], s60 offen offset:2064 lds\n"
      "buffer_load_dwordx4 v239, s[40:43], s60 offen offset:3096 lds\n"
      "s_add_u32 m0, s65, 0\n"
      "s_nop 0\n"
      "buffer_load_dwordx4 v242, s[32:35], s60 offen sc1 lds\n"
      "buffer_load_dwordx4 v242, s[32:35], s60 offen offset:1024 sc1 lds\n"
      "buffer_load_dwordx4 v242, s[32:35], s60 offen offset:2048 sc1 lds\n"
      "buffer_load_dwordx4 v242, s[32:35], s60 offen offset:3072 sc1 lds\n"
      "s_add_u32 m0, s77, 32896\n"
      "s_waitcnt vmcnt(9)\n"
      "s_barrier\n"
      "buffer_load_dwordx4 v236, s[40:43], s61 offen lds\n"
      "v_readlane_b32 s60, v241, 5\n"
      "buffer_load_dwordx4 v237, s[40:43], s61 offen offset:1032 lds\n"
      "buffer_load_dwordx4 v238, s[40:43], s61 offen offset:2064 lds\n"
      "buffer_load_dwordx4 v239, s[40:43], s61 offen offset:3096 lds\n"
      "s_add_u32 m0, s65, 32896\n"
      "s_nop 0\n"
      "buffer_load_dwordx4 v242, s[32:35], s61 offen sc1 lds\n"
      "buffer_load_dwordx4 v242, s[32:35], s61 offen offset:1024 sc1 lds\n"
      "buffer_load_dwordx4 v242, s[32:35], s61 offen offset:2048 sc1 lds\n"
      "buffer_load_dwordx4 v242, s[32:35], s61 offen offset:3072 sc1 lds\n"
      "s_add_u32 m0, s77, 65792\n"
      "s_waitcnt vmcnt(8)\n"
      "s_barrier\n"
      "buffer_load_dwordx4 v236, s[40:43], s60 offen lds\n"
      "v_readlane_b32 s61, v241, 6\n"
      "buffer_load_dwordx4 v237, s[40:43], s60 offen offset:1032 lds\n"
      "buffer_load_dwordx4 v238, s[40:43], s60 offen offset:2064 lds\n"
      "buffer_load_dwordx4 v239, s[40:43], s60 offen offset:3096 lds\n"
      "s_add_u32 m0, s65, 65792\n"
      "s_nop 0\n"
      "buffer_load_dwordx4 v242, s[32:35], s60 offen sc1 lds\n"
      "buffer_load_dwordx4 v242, s[32:35], s60 offen offset:1024 sc1 lds\n"
      "buffer_load_dwordx4 v242, s[32:35], s60 offen offset:2048 sc1 lds\n"
      "buffer_load_dwordx4 v242, s[32:35], s60 offen offset:3072 sc1 lds\n"
      "s_add_u32 m0, s77, 0\n"
      "s_waitcnt vmcnt(8)\n"
      "s_barrier\n"
      "buffer_load_dwordx4 v236, s[40:43], s61 offen lds\n"
      "v_readlane_b32 s60, v241, 7\n"
      "buffer_load_dwordx4 v237, s[40:43], s61 offen offset:1032 lds\n"
      "buffer_load_dwordx4 v238, s[40:43], s61 offen offset:2064 lds\n"
      "buffer_load_dwordx4 v239, s[40:43], s61 offen offset:3096 lds\n"
      "s_add_u32 m0, s65, 0\n"
      "s_nop 0\n"
      "buffer_load_dwordx4 v242, s[32:35], s61 offen sc1 lds\n"
      "buffer_load_dwordx4 v242, s[32:35], s61 offen offset:1024 sc1 lds\n"
      "buffer_load_dwordx4 v242, s[32:35], s61 offen offset:2048 sc1 lds\n"
      "buffer_load_dwordx4 v242, s[32:35], s61 offen offset:3072 sc1 lds\n"
      "s_add_u32 m0, s77, 32896\n"
      "s_waitcnt vmcnt(8)\n"
      "s_barrier\n"
      "buffer_load_dwordx2 v[192:193], v244, s[32:35], s58 offen sc1\n"
      "buffer_load_dwordx4 v236, s[40:43], s60 offen lds\n"
      "v_readlane_b32 s61, v241, 8\n"
      "buffer_load_dwordx4 v237, s[40:43], s60 offen offset:1032 lds\n"
      "buffer_load_dwordx4 v238, s[40:43], s60 offen offset:2064 lds\n"
      "buffer_load_dwordx4 v239, s[40:43], s60 offen offset:3096 lds\n"
      "s_add_u32 m0, s65, 32896\n"
      "s_nop 0\n"
      "buffer_load_dwordx4 v242, s[32:35], s60 offen sc1 lds\n"
      "buffer_load_dwordx4 v242, s[32:35], s60 offen offset:1024 sc1 lds\n"
      "buffer_load_dwordx4 v242, s[32:35], s60 offen offset:2048 sc1 lds\n"
      "buffer_load_dwordx4 v242, s[32:35], s60 offen offset:3072 sc1 lds\n"
      "s_add_u32 m0, s77, 65792\n"
      "s_waitcnt vmcnt(8)\n"
      "v_cmp_ne_u64 s[72:73], s[48:49], v[192:193]\n"
      "s_cmp_lg_u32 s29, 0\n"
      "s_cbranch_scc1 .Lk_h25\n"
      "s_cmp_eq_u64 s[72:73], 0\n"
      "s_cbranch_scc1 .Lk_h25\n"
      "s_mov_b32 s56, 60000\n"
      ".Lk_fw10:\n"
      "buffer_load_dwordx2 v[192:193], v244, s[32:35], s58 offen sc1\n"
      "s_waitcnt vmcnt(0)\n"
      "v_cmp_ne_u64 vcc, s[48:49], v[192:193]\n"
      "s_nop 0\n"
      "s_cbranch_vccz .Lk_fd11\n"
      "s_sleep 2\n"
      "s_sub_u32 s56, s56, 1\n"
      "s_cbranch_scc0 .Lk_fw10\n"
      ".Lk_fd11:\n"
      ".Lk_h25:\n"
      "s_add_u32 s58, s58, 64\n"
      "s_barrier\n"
      "buffer_load_dwordx4 v236, s[40:43], s61 offen lds\n"
      "v_readlane_b32 s60, v241, 9\n"
      "s_lshl_b32 s50, s28, 10\n"
      "s_lshl_b32 s51, s29, 8\n"
      "s_add_u32 s50, s50, s51\n"
      "buffer_load_dwordx4 v237, s[40:43], s61 offen offset:1032 lds\n"
      "buffer_load_dwordx4 v238, s[40:43], s61 offen offset:2064 lds\n"
      "s_add_u32 s50, s50, 151552\n"
      "v_and_b32 v15, 31, v250\n"
      "v_lshl_add_u32 v16, v15, 3, s50\n"
      "buffer_load_dwordx4 v239, s[40:43], s61 offen offset:3096 lds\n"
      "s_add_u32 m0, s65, 65792\n"
      "s_nop 0\n"
      "buffer_load_dwordx4 v242, s[32:35], s61 offen sc1 lds\n"
      "v_mov_b32 v17, s53\n"
      "v_cmp_gt_u32 vcc, 32, v250\n"
      "v_lshrrev_b32 v20, 3, v250\n"
      "buffer_load_dwordx4 v242, s[32:35], s61 offen offset:1024 sc1 lds\n"
      "buffer_load_dwordx4 v242, s[32:35], s61 offen offset:2048 sc1 lds\n"
      "buffer_load_dwordx4 v242, s[32:35], s61 offen offset:3072 sc1 lds\n"
      "s_add_u32 m0, s77, 0\n"
      "s_waitcnt vmcnt(8)\n"
      "s_barrier\n"
      "buffer_load_dwordx4 v236, s[40:43], s60 offen lds\n"
      "v_readlane_b32 s61, v241, 10\n"
      "v_and_b32 v21, 0x70, v253\n"
      "v_cndmask_b32 v16, v17, v16, vcc\n"
      "v_lshl_or_b32 v20, v20, 12, v21\n"
      "buffer_load_dwordx4 v237, s[40:43], s60 offen offset:1032 lds\n"
      "buffer_load_dwordx4 v238, s[40:43], s60 offen offset:2064 lds\n"
      "v_add_u32 v21, 31744, v20\n"
      "v_add_u32 v22, 63488, v20\n"
      "v_add_u32 v23, 95232, v20\n"
      "buffer_load_dwordx4 v239, s[40:43], s60 offen offset:3096 lds\n"
      "s_add_u32 m0, s65, 0\n"
      "s_nop 0\n"
      "buffer_load_dwordx4 v242, s[32:35], s60 offen sc1 lds\n"
      "s_lshl_b32 s69, s28, 19\n"
      "s_lshl_b32 s51, s29, 17\n"
      "s_add_u32 s69, s69, s51\n"
      "buffer_load_dwordx4 v242, s[32:35], s60 offen offset:1024 sc1 lds\n"
      "buffer_load_dwordx4 v242, s[32:35], s60 offen offset:2048 sc1 lds\n"
      "buffer_load_dwordx4 v242, s[32:35], s60 offen offset:3072 sc1 lds\n"
      "s_add_u32 m0, s77, 32896\n"
      "s_mov_b64 exec, 1\n"
      "buffer_store_dwordx2 v[248:249], off, s[32:35], s57 sc1\n"
      "s_mov_b64 exec, -1\n"
      "s_waitcnt vmcnt(9)\n"
      "s_barrier\n"
      "buffer_load_dwordx4 v236, s[40:43], s61 offen lds\n"
      "v_readlane_b32 s60, v241, 11\n"
      "s_lshl_b32 s51, s24, 8\n"
      "s_lshl_b32 s52, s27, 7\n"
      "s_add_u32 s51, s51, s52\n"
      "buffer_load_dwordx4 v237, s[40:43], s61 offen offset:1032 lds\n"
      "buffer_load_dwordx4 v238, s[40:43], s61 offen offset:2064 lds\n"
      "s_add_u32 s69, s69, s51\n"
      "s_add_u32 s69, s69, 8388608\n"
      "buffer_load_dwordx4 v239, s[40:43], s61 offen offset:3096 lds\n"
      "s_add_u32 m0, s65, 32896\n"
      "s_nop 0\n"
      "buffer_load_dwordx4 v242, s[32:35], s61 offen sc1 lds\n"
      "buffer_load_dwordx4 v242, s[32:35], s61 offen offset:1024 sc1 lds\n"
      "buffer_load_dwordx4 v242, s[32:35], s61 offen offset:2048 sc1 lds\n"
      "buffer_load_dwordx4 v242, s[32:35], s61 offen offset:3072 sc1 lds\n"
      "s_add_u32 m0, s77, 65792\n"
      "s_waitcnt vmcnt(9)\n"
      "s_barrier\n"
      "buffer_load_dwordx4 v236, s[40:43], s60 offen lds\n"
      "v_readlane_b32 s61, v241, 12\n"
      "buffer_load_dwordx4 v237, s[40:43], s60 offen offset:1032 lds\n"
      "buffer_load_dwordx4 v238, s[40:43], s60 offen offset:2064 lds\n"
      "buffer_load_dwordx4 v239, s[40:43], s60 offen offset:3096 lds\n"
      "s_add_u32 m0, s65, 65792\n"
      "s_nop 0\n"
      "buffer_load_dwordx4 v242, s[32:35], s60 offen sc1 lds\n"
      "buffer_load_dwordx4 v242, s[32:35], s60 offen offset:1024 sc1 lds\n"
      "buffer_load_dwordx4 v242, s[32:35], s60 offen offset:2048 sc1 lds\n"
      "buffer_load_dwordx4 v242, s[32:35], s60 offen offset:3072 sc1 lds\n"
      "s_add_u32 m0, s77, 0\n"
      "s_waitcnt vmcnt(8)\n"
      "s_barrier\n"
      "buffer_load_dwordx4 v236, s[40:43], s61 offen lds\n"
      "v_readlane_b32 s60, v241, 13\n"
      "buffer_load_dwordx4 v237, s[40:43], s61 offen offset:1032 lds\n"
      "buffer_load_dwordx4 v238, s[40:43], s61 offen offset:2064 lds\n"
      "buffer_load_dwordx4 v239, s[40:43], s61 offen offset:3096 lds\n"
      "s_add_u32 m0, s65, 0\n"
      "s_nop 0\n"
      "buffer_load_dwordx4 v242, s[32:35], s61 offen sc1 lds\n"
      "buffer_load_dwordx4 v242, s[32:35], s61 offen offset:1024 sc1 lds\n"
      "buffer_load_dwordx4 v242, s[32:35], s61 offen offset:2048 sc1 lds\n"
      "buffer_load_dwordx4 v242, s[32:35], s61 offen offset:3072 sc1 lds\n"
      "s_add_u32 m0, s77, 32896\n"
      "s_waitcnt vmcnt(8)\n"
      "s_barrier\n"
      "buffer_load_dwordx4 v236, s[40:43], s60 offen lds\n"
      "v_readlane_b32 s61, v241, 14\n"
      "buffer_load_dwordx4 v237, s[40:43], s60 offen offset:1032 lds\n"
      "buffer_load_dwordx4 v238, s[40:43], s60 offen offset:2064 lds\n"
      "buffer_load_dwordx4 v239, s[40:43], s60 offen offset:3096 lds\n"
      "s_add_u32 m0, s65, 32896\n"
      "s_nop 0\n"
      "buffer_load_dwordx4 v242, s[32:35], s60 offen sc1 lds\n"
      "buffer_load_dwordx4 v242, s[32:35], s60 offen offset:1024 sc1 lds\n"
      "buffer_load_dwordx4 v242, s[32:35], s60 offen offset:2048 sc1 lds\n"
      "buffer_load_dwordx4 v242, s[32:35], s60 offen offset:3072 sc1 lds\n"
      "s_add_u32 m0, s77, 65792\n"
      "s_waitcnt vmcnt(8)\n"
      "s_barrier\n"
      "buffer_load_dwordx4 v236, s[40:43], s61 offen lds\n"
      "v_readlane_b32 s60, v241, 15\n"
      "buffer_load_dwordx4 v237, s[40:43], s61 offen offset:1032 lds\n"
      "buffer_load_dwordx4 v238, s[40:43], s61 offen offset:2064 lds\n"
      "buffer_load_dwordx4 v239, s[40:43], s61 offen offset:3096 lds\n"
      "s_add_u32 m0, s65, 65792\n"
      "s_nop 0\n"
      "buffer_load_dwordx4 v242, s[32:35], s61 offen sc1 lds\n"
      "buffer_load_dwordx4 v242, s[32:35], s61 offen offset:1024 sc1 lds\n"
      "buffer_load_dwordx4 v242, s[32:35], s61 offen offset:2048 sc1 lds\n"
      "buffer_load_dwordx4 v242, s[32:35], s61 offen offset:3072 sc1 lds\n"
      "s_add_u32 m0, s77, 0\n"
      "s_waitcnt vmcnt(8)\n"
      "s_barrier\n"
      "buffer_load_dwordx4 v236, s[40:43], s60 offen lds\n"
      "v_readlane_b32 s61, v241, 16\n"
      "buffer_load_dwordx4 v237, s[40:43], s60 offen offset:1032 lds\n"
      "buffer_load_dwordx4 v238, s[40:43], s60 offen offset:2064 lds\n"
      "buffer_load_dwordx4 v239, s[40:43], s60 offen offset:3096 lds\n"
      "s_add_u32 m0, s65, 0\n"
      "s_nop 0\n"
      "buffer_load_dwordx4 v242, s[32:35], s60 offen sc1 lds\n"
      "buffer_load_dwordx4 v242, s[32:35], s60 offen offset:1024 sc1 lds\n"
      "buffer_load_dwordx4 v242, s[32:35], s60 offen offset:2048 sc1 lds\n"
      "buffer_load_dwordx4 v242, s[32:35], s60 offen offset:3072 sc1 lds\n"
      "s_add_u32 m0, s77, 32896\n"
      "s_waitcnt vmcnt(8)\n"
      "s_barrier\n"
      "buffer_load_dwordx4 v236, s[40:43], s61 offen lds\n"
      "v_readlane_b32 s60, v241, 17\n"
      "buffer_load_dwordx4 v237, s[40:43], s61 offen offset:1032 lds\n"
      "buffer_load_dwordx4 v238, s[40:43], s61 offen offset:2064 lds\n"
      "buffer_load_dwordx4 v239, s[40:43], s61 offen offset:3096 lds\n"
      "s_add_u32 m0, s65, 32896\n"
      "s_nop 0\n"
      "buffer_load_dwordx4 v242, s[32:35], s61 offen sc1 lds\n"
      "buffer_load_dwordx4 v242, s[32:35], s61 offen offset:1024 sc1 lds\n"
      "buffer_load_dwordx4 v242, s[32:35], s61 offen offset:2048 sc1 lds\n"
      "buffer_load_dwordx4 v242, s[32:35], s61 offen offset:3072 sc1 lds\n"
      "s_add_u32 m0, s77, 65792\n"
      "s_waitcnt vmcnt(8)\n"
      "s_barrier\n"
      "buffer_load_dwordx4 v236, s[40:43], s60 offen lds\n"
      "v_readlane_b32 s61, v241, 18\n"
      "buffer_load_dwordx4 v237, s[40:43], s60 offen offset:1032 lds\n"
      "buffer_load_dwordx4 v238, s[40:43], s60 offen offset:2064 lds\n"
      "buffer_load_dwordx4 v239, s[40:43], s60 offen offset:3096 lds\n"
      "s_add_u32 m0, s65, 65792\n"
      "s_nop 0\n"
      "buffer_load_dwordx4 v242, s[32:35], s60 offen sc1 lds\n"
      "buffer_load_dwordx4 v242, s[32:35], s60 offen offset:1024 sc1 lds\n"
      "buffer_load_dwordx4 v242, s[32:35], s60 offen offset:2048 sc1 lds\n"
      "buffer_load_dwordx4 v242, s[32:35], s60 offen offset:3072 sc1 lds\n"
      "s_add_u32 m0, s77, 0\n"
      "s_waitcnt vmcnt(8)\n"
      "s_barrier\n"
      "buffer_load_dwordx4 v236, s[40:43], s61 offen lds\n"
      "v_readlane_b32 s60, v241, 19\n"
      "buffer_load_dwordx4 v237, s[40:43], s61 offen offset:1032 lds\n"
      "buffer_load_dwordx4 v238, s[40:43], s61 offen offset:2064 lds\n"
      "buffer_load_dwordx4 v239, s[40:43], s61 offen offset:3096 lds\n"
      "s_add_u32 m0, s65, 0\n"
      "s_nop 0\n"
      "buffer_load_dwordx4 v242, s[32:35], s61 offen sc1 lds\n"
      "buffer_load_dwordx4 v242, s[32:35], s61 offen offset:1024 sc1 lds\n"
      "buffer_load_dwordx4 v242, s[32:35], s61 offen offset:2048 sc1 lds\n"
      "buffer_load_dwordx4 v242, s[32:35], s61 offen offset:3072 sc1 lds\n"
      "s_add_u32 m0, s77, 32896\n"
      "s_waitcnt vmcnt(8)\n"
      "s_barrier\n"
      "buffer_load_dwordx4 v236, s[40:43], s60 offen lds\n"
      "v_readlane_b32 s61, v241, 20\n"
      "buffer_load_dwordx4 v237, s[40:43], s60 offen offset:1032 lds\n"
      "buffer_load_dwordx4 v238, s[40:43], s60 offen offset:2064 lds\n"
      "buffer_load_dwordx4 v239, s[40:43], s60 offen offset:3096 lds\n"
      "s_add_u32 m0, s65, 32896\n"
      "s_nop 0\n"
      "buffer_load_dwordx4 v242, s[32:35], s60 offen sc1 lds\n"
      "buffer_load_dwordx4 v242, s[32:35], s60 offen offset:1024 sc1 lds\n"
      "buffer_load_dwordx4 v242, s[32:35], s60 offen offset:2048 sc1 lds\n"
      "buffer_load_dwordx4 v242, s[32:35], s60 offen offset:3072 sc1 lds\n"
      "s_add_u32 m0, s77, 65792\n"
      "s_waitcnt vmcnt(8)\n"
      "s_barrier\n"
      "buffer_load_dwordx4 v236, s[40:43], s61 offen lds\n"
      "v_readlane_b32 s60, v241, 21\n"
      "buffer_load_dwordx4 v237, s[40:43], s61 offen offset:1032 lds\n"
      "buffer_load_dwordx4 v238, s[40:43], s61 offen offset:2064 lds\n"
      "buffer_load_dwordx4 v239, s[40:43], s61 offen offset:3096 lds\n"
      "s_add_u32 m0, s65, 65792\n"
      "s_nop 0\n"
      "buffer_load_dwordx4 v242, s[32:35], s61 offen sc1 lds\n"
      "buffer_load_dwordx4 v242, s[32:35], s61 offen offset:1024 sc1 lds\n"
      "buffer_load_dwordx4 v242, s[32:35], s61 offen offset:2048 sc1 lds\n"
      "buffer_load_dwordx4 v242, s[32:35], s61 offen offset:3072 sc1 lds\n"
      "s_add_u32 m0, s77, 0\n"
      "s_waitcnt vmcnt(8)\n"
      "s_barrier\n"
      "buffer_load_dwordx4 v236, s[40:43], s60 offen lds\n"
      "v_readlane_b32 s61, v241, 22\n"
      "buffer_load_dwordx4 v237, s[40:43], s60 offen offset:1032 lds\n"
      "buffer_load_dwordx4 v238, s[40:43], s60 offen offset:2064 lds\n"
      "buffer_load_dwordx4 v239, s[40:43], s60 offen offset:3096 lds\n"
      "s_add_u32 m0, s65, 0\n"
      "s_nop 0\n"
      "buffer_load_dwordx4 v242, s[32:35], s60 offen sc1 lds\n"
      "buffer_load_dwordx4 v242, s[32:35], s60 offen offset:1024 sc1 lds\n"
      "buffer_load_dwordx4 v242, s[32:35], s60 offen offset:2048 sc1 lds\n"
      "buffer_load_dwordx4 v242, s[32:35], s60 offen offset:3072 sc1 lds\n"
      "s_add_u32 m0, s77, 32896\n"
      "s_waitcnt vmcnt(8)\n"
      "s_barrier\n"
      "s_mov_b64 exec, 7\n"
      "global_load_dword v51, v243, s[84:85]\n"
      "s_mov_b64 exec, -1\n"
      "buffer_load_dwordx4 v236, s[40:43], s61 offen lds\n"
      "v_readlane_b32 s60, v241, 23\n"
      "buffer_load_dwordx4 v237, s[40:43], s61 offen offset:1032 lds\n"
      "buffer_load_dwordx4 v238, s[40:43], s61 offen offset:2064 lds\n"
      "buffer_load_dwordx4 v239, s[40:43], s61 offen offset:3096 lds\n"
      "s_add_u32 m0, s65, 32896\n"
      "s_nop 0\n"
      "buffer_load_dwordx4 v242, s[32:35], s61 offen sc1 lds\n"
      "buffer_load_dwordx4 v242, s[32:35], s61 offen offset:1024 sc1 lds\n"
      "buffer_load_dwordx4 v242, s[32:35], s61 offen offset:2048 sc1 lds\n"
      "buffer_load_dwordx4 v242, s[32:35], s61 offen offset:3072 sc1 lds\n"
      "s_add_u32 m0, s77, 65792\n"
      "s_waitcnt vmcnt(9)\n"
      "s_barrier\n"
      "buffer_load_dwordx4 v236, s[40:43], s60 offen lds\n"
      "v_readlane_b32 s61, v241, 24\n"
      "buffer_load_dwordx4 v237, s[40:43], s60 offen offset:1032 lds\n"
      "buffer_load_dwordx4 v238, s[40:43], s60 offen offset:2064 lds\n"
      "buffer_load_dwordx4 v239, s[40:43], s60 offen offset:3096 lds\n"
      "s_add_u32 m0, s65, 65792\n"
      "s_nop 0\n"
      "buffer_load_dwordx4 v242, s[32:35], s60 offen sc1 lds\n"
      "buffer_load_dwordx4 v242, s[32:35], s60 offen offset:1024 sc1 lds\n"
      "buffer_load_dwordx4 v242, s[32:35], s60 offen offset:2048 sc1 lds\n"
      "buffer_load_dwordx4 v242, s[32:35], s60 offen offset:3072 sc1 lds\n"
      "s_add_u32 m0, s77, 0\n"
      "s_waitcnt vmcnt(8)\n"
      "s_barrier\n"
      "buffer_load_dwordx4 v236, s[40:43], s61 offen lds\n"
      "v_readlane_b32 s60, v241, 25\n"
      "buffer_load_dwordx4 v237, s[40:43], s61 offen offset:1032 lds\n"
      "buffer_load_dwordx4 v238, s[40:43], s61 offen offset:2064 lds\n"
      "buffer_load_dwordx4 v239, s[40:43], s61 offen offset:3096 lds\n"
      "s_add_u32 m0, s65, 0\n"
      "s_nop 0\n"
      "buffer_load_dwordx4 v242, s[32:35], s61 offen sc1 lds\n"
      "buffer_load_dwordx4 v242, s[32:35], s61 offen offset:1024 sc1 lds\n"
      "buffer_load_dwordx4 v242, s[32:35], s61 offen offset:2048 sc1 lds\n"
      "buffer_load_dwordx4 v242, s[32:35], s61 offen offset:3072 sc1 lds\n"
      "s_add_u32 m0, s77, 32896\n"
      "s_waitcnt vmcnt(8)\n"
      "s_barrier\n"
      "buffer_load_dwordx2 v[18:19], v16, s[32:35], 0 offen sc1\n"
      "buffer_load_dwordx4 v236, s[40:43], s60 offen lds\n"
      "v_readlane_b32 s61, v241, 26\n"
      "buffer_load_dwordx4 v237, s[40:43], s60 offen offset:1032 lds\n"
      "buffer_load_dwordx4 v238, s[40:43], s60 offen offset:2064 lds\n"
      "buffer_load_dwordx4 v239, s[40:43], s60 offen offset:3096 lds\n"
      "s_add_u32 m0, s65, 32896\n"
      "s_nop 0\n"
      "buffer_load_dwordx4 v242, s[32:35], s60 offen sc1 lds\n"
      "buffer_load_dwordx4 v242, s[32:35], s60 offen offset:1024 sc1 lds\n"
      "buffer_load_dwordx4 v242, s[32:35], s60 offen offset:2048 sc1 lds\n"
      "buffer_load_dwordx4 v242, s[32:35], s60 offen offset:3072 sc1 lds\n"
      "s_add_u32 m0, s77, 65792\n"
      "s_waitcnt vmcnt(9)\n"
      "s_barrier\n"
      "buffer_load_dwordx4 v236, s[40:43], s61 offen lds\n"
      "v_readlane_b32 s60, v241, 27\n"
      "buffer_load_dwordx4 v237, s[40:43], s61 offen offset:1032 lds\n"
      "buffer_load_dwordx4 v238, s[40:43], s61 offen offset:2064 lds\n"
      "buffer_load_dwordx4 v239, s[40:43], s61 offen offset:3096 lds\n"
      "s_add_u32 m0, s65, 65792\n"
      "s_nop 0\n"
      "buffer_load_dwordx4 v242, s[32:35], s61 offen sc1 lds\n"
      "buffer_load_dwordx4 v242, s[32:35], s61 offen offset:1024 sc1 lds\n"
      "buffer_load_dwordx4 v242, s[32:35], s61 offen offset:2048 sc1 lds\n"
      "buffer_load_dwordx4 v242, s[32:35], s61 offen offset:3072 sc1 lds\n"
      "s_add_u32 m0, s77, 0\n"
      "s_waitcnt vmcnt(8)\n"
      "s_barrier\n"
      "buffer_load_dwordx4 v236, s[40:43], s60 offen lds\n"
      "buffer_load_dwordx4 v237, s[40:43], s60 offen offset:1032 lds\n"
      "buffer_load_dwordx4 v238, s[40:43], s60 offen offset:2064 lds\n"
      "buffer_load_dwordx4 v239, s[40:43], s60 offen offset:3096 lds\n"
      "s_add_u32 m0, s65, 0\n"
      "s_nop 0\n"
      "buffer_load_dwordx4 v242, s[32:35], s60 offen sc1 lds\n"
      "buffer_load_dwordx4 v242, s[32:35], s60 offen offset:1024 sc1 lds\n"
      "buffer_load_dwordx4 v242, s[32:35], s60 offen offset:2048 sc1 lds\n"
      "buffer_load_dwordx4 v242, s[32:35], s60 offen offset:3072 sc1 lds\n"
      "s_waitcnt vmcnt(8)\n"
      "s_barrier\n"
      "v_cmp_ne_u64 vcc, s[48:49], v[18:19]\n"
      "s_nop 0\n"
      "s_cbranch_vccz .Lk_yok\n"
      "s_mov_b32 s56, 60000\n"
      ".Lk_fw12:\n"
      "buffer_load_dwordx2 v[18:19], v16, s[32:35], 0 offen sc1\n"
      "s_waitcnt vmcnt(0)\n"
      "v_cmp_ne_u64 vcc, s[48:49], v[18:19]\n"
      "s_nop 0\n"
      "s_cbranch_vccz .Lk_fd13\n"
      "s_sleep 4\n"
      "s_sub_u32 s56, s56, 1\n"
      "s_cbranch_scc0 .Lk_fw12\n"
      ".Lk_fd13:\n"
      ".Lk_yok:\n"
      "s_add_u32 m0, s65, 85888\n"
      "s_nop 0\n"
      "buffer_load_dwordx4 v20, s[32:35], s69 offen sc1 lds\n"
      "s_add_u32 m0, s65, 85888\n"
      "s_nop 0\n"
      "buffer_load_dwordx4 v21, s[32:35], s69 offen offset:1024 sc1 lds\n"
      "s_add_u32 m0, s65, 85888\n"
      "s_nop 0\n"
      "buffer_load_dwordx4 v22, s[32:35], s69 offen offset:2048 sc1 lds\n"
      "s_add_u32 m0, s65, 85888\n"
      "s_nop 0\n"
      "buffer_load_dwordx4 v23, s[32:35], s69 offen offset:3072 sc1 lds\n"
      "s_waitcnt vmcnt(4)\n"
      "s_barrier\n"
      "s_waitcnt vmcnt(0)\n"
      "s_barrier\n"
      "s_lshl_b32 s50, s30, 2\n"
      "s_add_u32 s50, s50, s29\n"
      "s_cmp_eq_u32 s50, 0\n"
      "s_cbranch_scc0 .Lk_l2d\n"
      "v_lshlrev_b32 v20, 3, v250\n"
      "s_mov_b32 s56, 60000\n"
      ".Lk_ep:\n"
      "s_mov_b32 s59, 4096\n"
      "buffer_load_dwordx2 v[22:23], v20, s[32:35], s59 offen sc1\n"
      "s_mov_b32 s59, 4608\n"
      "buffer_load_dwordx2 v[24:25], v20, s[32:35], s59 offen sc1\n"
      "s_mov_b32 s59, 5120\n"
      "buffer_load_dwordx2 v[26:27], v20, s[32:35], s59 offen sc1\n"
      "s_mov_b32 s59, 5632\n"
      "buffer_load_dwordx2 v[28:29], v20, s[32:35], s59 offen sc1\n"
      "s_waitcnt vmcnt(0)\n"
      "v_cmp_ne_u64 s[66:67], s[48:49], v[22:23]\n"
      "v_cmp_ne_u64 s[68:69], s[48:49], v[24:25]\n"
      "v_cmp_ne_u64 s[70:71], s[48:49], v[26:27]\n"
      "v_cmp_ne_u64 s[72:73], s[48:49], v[28:29]\n"
      "s_or_b64 s[66:67], s[66:67], s[68:69]\n"
      "s_or_b64 s[70:71], s[70:71], s[72:73]\n"
      "s_or_b64 s[66:67], s[66:67], s[70:71]\n"
      "s_cbranch_scc0 .Lk_epd\n"
      "s_sub_u32 s56, s56, 1\n"
      "s_cbranch_scc0 .Lk_ep\n"
      ".Lk_epd:\n"
      "s_mov_b32 s59, 0\n"
      "s_mov_b64 exec, 1\n"
      "buffer_store_dwordx2 v[248:249], off, s[32:35], s59 sc1\n"
      "s_mov_b64 exec, -1\n"
      ".Lk_l2d:\n"
      "s_endpgm\n"
      ".Lk_xg0:\n"
      "v_add_u32 v150, 45056, v149\n"
      "v_xor_b32 v151, 64, v150\n"
      "ds_read_b128 v[176:179], v150 offset:0\n"
      "ds_read_b128 v[180:183], v151 offset:0\n"
      "ds_read_b128 v[184:187], v150 offset:2048\n"
      "ds_read_b128 v[188:191], v151 offset:2048\n"
      "s_waitcnt lgkmcnt(0)\n"
      "s_branch .Lk_xr0\n"
      ".Lk_xg1:\n"
      "v_add_u32 v150, 90112, v149\n"
      "v_xor_b32 v151, 64, v150\n"
      "ds_read_b128 v[176:179], v150 offset:0\n"
      "ds_read_b128 v[180:183], v151 offset:0\n"
      "ds_read_b128 v[184:187], v150 offset:2048\n"
      "ds_read_b128 v[188:191], v151 offset:2048\n"
      "s_waitcnt lgkmcnt(0)\n"
      "s_branch .Lk_xr1\n"
      ".Lk_xg2:\n"
      "v_add_u32 v150, 0, v149\n"
      "v_xor_b32 v151, 64, v150\n"
      "ds_read_b128 v[176:179], v150 offset:0\n"
      "ds_read_b128 v[180:183], v151 offset:0\n"
      "ds_read_b128 v[184:187], v150 offset:2048\n"
      "ds_read_b128 v[188:191], v151 offset:2048\n"
      "s_waitcnt lgkmcnt(0)\n"
      "s_branch .Lk_xr2\n"
      ".Lk_xg3:\n"
      "v_add_u32 v150, 45056, v149\n"
      "v_xor_b32 v151, 64, v150\n"
      "ds_read_b128 v[176:179], v150 offset:0\n"
      "ds_read_b128 v[180:183], v151 offset:0\n"
      "ds_read_b128 v[184:187], v150 offset:2048\n"
      "ds_read_b128 v[188:191], v151 offset:2048\n"
      "s_waitcnt lgkmcnt(0)\n"
      "s_branch .Lk_xr3\n"
      ".Lk_xg4:\n"
      "v_add_u32 v150, 90112, v149\n"
      "v_xor_b32 v151, 64, v150\n"
      "ds_read_b128 v[176:179], v150 offset:0\n"
      "ds_read_b128 v[180:183], v151 offset:0\n"
      "ds_read_b128 v[184:187], v150 offset:2048\n"
      "ds_read_b128 v[188:191], v151 offset:2048\n"
      "s_waitcnt lgkmcnt(0)\n"
      "s_branch .Lk_xr4\n"
      ".Lk_xg5:\n"
      "v_add_u32 v150, 0, v149\n"
      "v_xor_b32 v151, 64, v150\n"
      "ds_read_b128 v[176:179], v150 offset:0\n"
      "ds_read_b128 v[180:183], v151 offset:0\n"
      "ds_read_b128 v[184:187], v150 offset:2048\n"
      "ds_read_b128 v[188:191], v151 offset:2048\n"
      "s_waitcnt lgkmcnt(0)\n"
      "s_branch .Lk_xr5\n"
      ".Lk_xg6:\n"
      "v_add_u32 v150, 45056, v149\n"
      "v_xor_b32 v151, 64, v150\n"
      "ds_read_b128 v[176:179], v150 offset:0\n"
      "ds_read_b128 v[180:183], v151 offset:0\n"
      "ds_read_b128 v[184:187], v150 offset:2048\n"
      "ds_read_b128 v[188:191], v151 offset:2048\n"
      "s_waitcnt lgkmcnt(0)\n"
      "s_branch .Lk_xr6\n"
      ".Lk_xg7:\n"
      "v_add_u32 v150, 90112, v149\n"
      "v_xor_b32 v151, 64, v150\n"
      "ds_read_b128 v[176:179], v150 offset:0\n"
      "ds_read_b128 v[180:183], v151 offset:0\n"
      "ds_read_b128 v[184:187], v150 offset:2048\n"
      "ds_read_b128 v[188:191], v151 offset:2048\n"
      "s_waitcnt lgkmcnt(0)\n"
      "s_branch .Lk_xr7\n"
      ".Lk_xg8:\n"
      "v_add_u32 v150, 0, v149\n"
      "v_xor_b32 v151, 64, v150\n"
      "ds_read_b128 v[176:179], v150 offset:0\n"
      "ds_read_b128 v[180:183], v151 offset:0\n"
      "ds_read_b128 v[184:187], v150 offset:2048\n"
      "ds_read_b128 v[188:191], v151 offset:2048\n"
      "s_waitcnt lgkmcnt(0)\n"
      "s_branch .Lk_xr8\n"
      ".Lk_xg9:\n"
      "v_add_u32 v150, 45056, v149\n"
      "v_xor_b32 v151, 64, v150\n"
      "ds_read_b128 v[176:179], v150 offset:0\n"
      "ds_read_b128 v[180:183], v151 offset:0\n"
      "ds_read_b128 v[184:187], v150 offset:2048\n"
      "ds_read_b128 v[188:191], v151 offset:2048\n"
      "s_waitcnt lgkmcnt(0)\n"
      "s_branch .Lk_xr9\n"
      ".Lk_xg10:\n"
      "v_add_u32 v150, 90112, v149\n"
      "v_xor_b32 v151, 64, v150\n"
      "ds_read_b128 v[176:179], v150 offset:0\n"
      "ds_read_b128 v[180:183], v151 offset:0\n"
      "ds_read_b128 v[184:187], v150 offset:2048\n"
      "ds_read_b128 v[188:191], v151 offset:2048\n"
      "s_waitcnt lgkmcnt(0)\n"
      "s_branch .Lk_xr10\n"
      ".Lk_xg11:\n"
      "v_add_u32 v150, 0, v149\n"
      "v_xor_b32 v151, 64, v150\n"
      "ds_read_b128 v[176:179], v150 offset:0\n"
      "ds_read_b128 v[180:183], v151 offset:0\n"
      "ds_read_b128 v[184:187], v150 offset:2048\n"
      "ds_read_b128 v[188:191], v151 offset:2048\n"
      "s_waitcnt lgkmcnt(0)\n"
      "s_branch .Lk_xr11\n"
      ".Lk_xg12:\n"
      "v_add_u32 v150, 45056, v149\n"
      "v_xor_b32 v151, 64, v150\n"
      "ds_read_b128 v[176:179], v150 offset:0\n"
      "ds_read_b128 v[180:183], v151 offset:0\n"
      "ds_read_b128 v[184:187], v150 offset:2048\n"
      "ds_read_b128 v[188:191], v151 offset:2048\n"
      "s_waitcnt lgkmcnt(0)\n"
      "s_branch .Lk_xr12\n"
      ".Lk_xg13:\n"
      "v_add_u32 v150, 90112, v149\n"
      "v_xor_b32 v151, 64, v150\n"
      "ds_read_b128 v[176:179], v150 offset:0\n"
      "ds_read_b128 v[180:183], v151 offset:0\n"
      "ds_read_b128 v[184:187], v150 offset:2048\n"
      "ds_read_b128 v[188:191], v151 offset:2048\n"
      "s_waitcnt lgkmcnt(0)\n"
      "s_branch .Lk_xr13\n"
      ".Lk_xg14:\n"
      "v_add_u32 v150, 0, v149\n"
      "v_xor_b32 v151, 64, v150\n"
      "ds_read_b128 v[176:179], v150 offset:0\n"
      "ds_read_b128 v[180:183], v151 offset:0\n"
      "ds_read_b128 v[184:187], v150 offset:2048\n"
      "ds_read_b128 v[188:191], v151 offset:2048\n"
      "s_waitcnt lgkmcnt(0)\n"
      "s_branch .Lk_xr14\n"
      ".Lk_xg15:\n"
      "v_add_u32 v150, 45056, v149\n"
      "v_xor_b32 v151, 64, v150\n"
      "ds_read_b128 v[176:179], v150 offset:0\n"
      "ds_read_b128 v[180:183], v151 offset:0\n"
      "ds_read_b128 v[184:187], v150 offset:2048\n"
      "ds_read_b128 v[188:191], v151 offset:2048\n"
      "s_waitcnt lgkmcnt(0)\n"
      "s_branch .Lk_xr15\n"
      ".Lk_end:\n"
      :
      : "{s[0:1]}"(__builtin_amdgcn_kernarg_segment_ptr()), "{s2}"(__builtin_amdgcn_workgroup_id_x()), "{v0}"(__builtin_amdgcn_workitem_id_x())
      : "v255", "s95", "vcc", "scc", "memory");
}

extern "C" void kernel_launch(void* const* d_in, const int* in_sizes, int n_in, void* d_out, int out_size, void* d_ws, size_t ws_size,
                              hipStream_t stream) {
  (void)hipFuncSetAttribute((const void*)fused_kernel, hipFuncAttributeMaxDynamicSharedMemorySize, 163840);
  fused_kernel<<<256, 512, 163840, stream>>>(d_in[0], d_in[1], d_in[2], d_in[3], d_in[4], d_in[5], d_in[7], d_in[8], d_out, d_ws);
}
```
